```python
import torch

def _softmax(x, axis=-1):
    return torch.softmax(x, dim=axis)
def _gelu(x):
    return torch.nn.functional.gelu(x, approximate="tanh")
def _silu(x):
    return torch.nn.functional.silu(x)
def _sigmoid(x):
    return torch.sigmoid(x)
def _layernorm(x, axis=-1, eps=1e-5):
    m = x.mean(dim=axis, keepdim=True)
    v = x.var(dim=axis, keepdim=True, unbiased=False)
    return (x - m) / torch.sqrt(v + eps)
def _scatter_add(tgt, idx, src, axis=0):
    return tgt.clone().index_add_(axis, idx, src)
def _segment_sum(src, seg, n):
    out = torch.zeros((n,) + src.shape[1:], dtype=src.dtype)
    return out.index_add_(0, seg, src)
def _pad(x, pw):
    flat = tuple(v for pair in reversed(pw) for v in pair)
    return torch.nn.functional.pad(x, flat)

kernel_name = 'interleave_layers__gqa_block_cached__compressed_kv_cla__decode'

def setup_inputs():

    _w = (lambda a: (lambda t: t.to(torch.bfloat16) if t.is_floating_point() else t)(torch.as_tensor(a)))
    def _A_setup():
        B, Sq, D = 1, 1, 640
        Hq, Hkv, Sk, Dh = 32, 32, 4096, 192
        assert Hq % Hkv == 0, (Hq, Hkv)
        return {
            "x": _w((lambda *s: torch.randn(*s, dtype=torch.bfloat16) * 0.05)(B, Sq, D)),
            "k_cache": _w((lambda *s: torch.randn(*s, dtype=torch.bfloat16) * 0.8)(B, Hkv, Sk, Dh)),
            "v_cache": _w((lambda *s: torch.randn(*s, dtype=torch.bfloat16) * 0.8)(B, Hkv, Sk, Dh)),
            "wq": _w((lambda *s: torch.randn(*s, dtype=torch.bfloat16) * 0.8)(D, Hq * Dh) * (D ** -0.5)),
            "wo": _w((lambda *s: torch.randn(*s, dtype=torch.bfloat16) * 0.8)(Hq * Dh, D) * ((Hq * Dh) ** -0.5)),
        }
    _A_in = _A_setup()

    def _B_setup():
        B, H, Sq, Sk, D, M = 1, 32, 1, 4096, 192, 640
        Dc = 4 * D
        return {
            "x": _w((lambda *s: torch.randn(*s, dtype=torch.bfloat16) * 0.05)(B, Sq, M)),
            "c_kv": _w((lambda *s: torch.randn(*s, dtype=torch.bfloat16) * 0.4)(B, Sk, Dc)),
            "w_q": _w((lambda *s: torch.randn(*s, dtype=torch.bfloat16) * 3.2)(M, H, D) * (M ** -0.5)),
            "w_uk": _w((lambda *s: torch.randn(*s, dtype=torch.bfloat16) * 3.2)(Dc, H, D) * (Dc ** -0.5)),
            "w_uv": _w((lambda *s: torch.randn(*s, dtype=torch.bfloat16) * 0.4)(Dc, H, D) * (Dc ** -0.5)),
            "w_o": _w((lambda *s: torch.randn(*s, dtype=torch.bfloat16) * 0.4)(H, D, M) * ((H * D) ** -0.5)),
        }
    _B_in = _B_setup()

    return {
        "A_x": _A_in["x"],
        "A_k_cache": _A_in["k_cache"],
        "A_v_cache": _A_in["v_cache"],
        "A_wq": _A_in["wq"],
        "A_wo": _A_in["wo"],
        "B_x": _B_in["x"],
        "B_c_kv": _B_in["c_kv"],
        "B_w_q": _B_in["w_q"],
        "B_w_uk": _B_in["w_uk"],
        "B_w_uv": _B_in["w_uv"],
        "B_w_o": _B_in["w_o"],
    }

def reference(A_x, A_k_cache, A_v_cache, A_wq, A_wo, B_x, B_c_kv, B_w_q, B_w_uk, B_w_uv, B_w_o):
    def _A_ref(x, k_cache, v_cache, wq, wo):
        B, Sq, D = x.shape
        Hq, Dh = 32, 192
        Hkv = k_cache.shape[1]
        rep = Hq // Hkv
        q = (lambda x, *d: x.permute(*d))((x @ wq).reshape(B, Sq, Hq, Dh), 0, 2, 1, 3)
        k = (lambda x, n, axis: torch.repeat_interleave(x, n, dim=axis))(k_cache, rep, axis=1)
        v = (lambda x, n, axis: torch.repeat_interleave(x, n, dim=axis))(v_cache, rep, axis=1)
        scores = torch.einsum("bhqd,bhkd->bhqk", q, k) * (Dh ** -0.5)
        p = _softmax(scores, axis=-1)
        o = torch.einsum("bhqk,bhkd->bhqd", p, v)
        return (lambda x, *d: x.permute(*d))(o, 0, 2, 1, 3).reshape(B, Sq, Hq * Dh) @ wo

    def _B_ref(x, c_kv, w_q, w_uk, w_uv, w_o):
        B, Sq, M = x.shape[0], x.shape[1], x.shape[2]
        Sk = c_kv.shape[1]
        H, D = w_q.shape[1], w_q.shape[2]
        q = torch.einsum("bqm,mhd->bhqd", x, w_q)
        k = torch.einsum("bkc,chd->bhkd", c_kv, w_uk)
        v = torch.einsum("bkc,chd->bhkd", c_kv, w_uv)
        scores = torch.einsum("bhqd,bhkd->bhqk", q, k) * (D ** -0.5)
        off = Sk - Sq
        qi = torch.arange(Sq)[:, None]
        kj = torch.arange(Sk)[None, :]
        cmask = kj <= (qi + off)
        scores = torch.where(cmask[None, None], scores, -1e9)
        p = _softmax(scores, axis=-1)
        ctx = torch.einsum("bhqk,bhkd->bhqd", p, v)
        return torch.einsum("bhqd,hdm->bqm", ctx, w_o)

    x = A_x
    x = x + _A_ref(_layernorm(x, axis=-1), A_k_cache, A_v_cache, A_wq, A_wo)
    x = x + _B_ref(_layernorm(x, axis=-1), B_c_kv, B_w_q, B_w_uk, B_w_uv, B_w_o)
    x = x + _A_ref(_layernorm(x, axis=-1), A_k_cache, A_v_cache, A_wq, A_wo)
    x = x + _B_ref(_layernorm(x, axis=-1), B_c_kv, B_w_q, B_w_uk, B_w_uv, B_w_o)
    return x
```

```cpp
#include <hip/hip_runtime.h>
#include <hip/hip_bf16.h>
#include <stdint.h>

typedef unsigned short u16;
__device__ __forceinline__ float bf2f(u16 b){ return __builtin_bit_cast(float, (unsigned)b<<16); }
__device__ __forceinline__ u16 f2bf(float f){ unsigned u=__builtin_bit_cast(unsigned,f); u += 0x7fffu + ((u>>16)&1u); return (u16)(u>>16); }
__device__ __forceinline__ float rbf(float f){ return bf2f(f2bf(f)); }

constexpr int DM=640, H=32, DH=192, SK=4096, DC=768;

__device__ float block_sum(float v, float* sh){
  for(int o=32;o;o>>=1) v+=__shfl_xor(v,o);
  int w=threadIdx.x>>6; __syncthreads();
  if((threadIdx.x&63)==0) sh[w]=v; __syncthreads();
  float s=0; for(int i=0;i<(int)(blockDim.x>>6);++i) s+=sh[i];
  return s;
}
__device__ float block_max(float v, float* sh){
  for(int o=32;o;o>>=1) v=fmaxf(v,__shfl_xor(v,o));
  int w=threadIdx.x>>6; __syncthreads();
  if((threadIdx.x&63)==0) sh[w]=v; __syncthreads();
  float s=-INFINITY; for(int i=0;i<(int)(blockDim.x>>6);++i) s=fmaxf(s,sh[i]);
  return s;
}

__global__ void k_init(const u16* x, float* xf){ int i=threadIdx.x+blockIdx.x*blockDim.x; if(i<DM) xf[i]=bf2f(x[i]); }

__global__ void k_lnq(const float* xf, const u16* W, float* q){
  __shared__ float xn[DM]; __shared__ float sh[8]; __shared__ float part[4][64];
  float s=0; for(int i=threadIdx.x;i<DM;i+=256) s+=xf[i];
  float mean=block_sum(s,sh)/DM;
  float v=0; for(int i=threadIdx.x;i<DM;i+=256){ float d=xf[i]-mean; v+=d*d; }
  float var=block_sum(v,sh)/DM;
  float rs=1.0f/sqrtf(var+1e-5f);
  for(int i=threadIdx.x;i<DM;i+=256) xn[i]=rbf((xf[i]-mean)*rs);
  __syncthreads();
  int lane=threadIdx.x&63,w=threadIdx.x>>6; int n=blockIdx.x*64+lane;
  float a=0; for(int m=w*160;m<w*160+160;++m) a+=xn[m]*bf2f(W[(size_t)m*6144+n]);
  part[w][lane]=a; __syncthreads();
  if(w==0) q[n]=rbf(part[0][lane]+part[1][lane]+part[2][lane]+part[3][lane]);
}
template<int L>
__global__ void k_attn(const float* q, const u16* K, const u16* V, size_t hstride, float scale, float* po, float* pm, float* pl){
  __shared__ float qs[L]; __shared__ float p[512]; __shared__ float sh[8];
  int h=blockIdx.x>>3, s=blockIdx.x&7;
  for(int i=threadIdx.x;i<L;i+=256) qs[i]=q[h*L+i];
  __syncthreads();
  const u16* Kh=K+h*hstride+(size_t)s*512*L; const u16* Vh=V+h*hstride+(size_t)s*512*L;
  int lane=threadIdx.x&63,w=threadIdx.x>>6;
  for(int k=w;k<512;k+=4){ float a=0; for(int d=lane;d<L;d+=64) a+=qs[d]*bf2f(Kh[(size_t)k*L+d]);
    for(int o=32;o;o>>=1) a+=__shfl_xor(a,o); if(lane==0) p[k]=a*scale; }
  __syncthreads();
  float m=fmaxf(p[threadIdx.x],p[threadIdx.x+256]); m=block_max(m,sh);
  float e0=__expf(p[threadIdx.x]-m), e1=__expf(p[threadIdx.x+256]-m);
  __syncthreads(); p[threadIdx.x]=e0; p[threadIdx.x+256]=e1;
  float l=block_sum(e0+e1,sh);
  __syncthreads();
  for(int d=threadIdx.x;d<L;d+=256){ float a=0; for(int k=0;k<512;++k) a+=p[k]*bf2f(Vh[(size_t)k*L+d]); po[(size_t)blockIdx.x*L+d]=a; }
  if(threadIdx.x==0){ pm[blockIdx.x]=m; pl[blockIdx.x]=l; }
}
template<int L>
__global__ void k_comb(const float* po,const float* pm,const float* pl,float* o){
  int h=blockIdx.x; float m=-INFINITY; for(int s=0;s<8;++s) m=fmaxf(m,pm[h*8+s]);
  float l=0; for(int s=0;s<8;++s) l+=pl[h*8+s]*__expf(pm[h*8+s]-m);
  for(int d=threadIdx.x;d<L;d+=blockDim.x){ float a=0; for(int s=0;s<8;++s) a+=po[(size_t)(h*8+s)*L+d]*__expf(pm[h*8+s]-m); o[h*L+d]=a/l; }
}
__global__ void k_out(const float* o,const u16* W,float* xf,u16* out){
  __shared__ float part[16][64];
  int lane=threadIdx.x&63,w=threadIdx.x>>6; int n=blockIdx.x*64+lane;
  float a=0; for(int k=w*384;k<w*384+384;++k) a+=rbf(o[k])*bf2f(W[(size_t)k*DM+n]);
  part[w][lane]=a; __syncthreads();
  if(w==0){ float s=0; for(int i=0;i<16;++i) s+=part[i][lane]; float r=rbf(xf[n]+rbf(s)); xf[n]=r; if(out) out[n]=f2bf(r);}
}
__global__ void k_qabs(const float* q,const u16* wuk,float* qa){
  int wv=(blockIdx.x*256+threadIdx.x)>>6, lane=threadIdx.x&63; int c=wv/H,h=wv%H;
  float a=0; for(int d=lane;d<DH;d+=64) a+=q[h*DH+d]*bf2f(wuk[((size_t)c*H+h)*DH+d]);
  for(int o=32;o;o>>=1) a+=__shfl_xor(a,o); if(lane==0) qa[h*DC+c]=a;
}
__global__ void k_ctx(const float* cl,const u16* wuv,float* ctx){
  __shared__ float part[4][64];
  int lane=threadIdx.x&63,w=threadIdx.x>>6; int n=blockIdx.x*64+lane; int h=n/DH;
  float a=0; for(int c=w*192;c<w*192+192;++c) a+=cl[h*DC+c]*bf2f(wuv[(size_t)c*6144+n]);
  part[w][lane]=a; __syncthreads();
  if(w==0) ctx[n]=part[0][lane]+part[1][lane]+part[2][lane]+part[3][lane];
}

extern "C" void kernel_launch(void* const* d_in, const int* in_sizes, int n_in,
                              void* d_out, int out_size, void* d_ws, size_t ws_size,
                              hipStream_t stream) {
  const u16* Ax=(const u16*)d_in[0]; const u16* Kc=(const u16*)d_in[1]; const u16* Vc=(const u16*)d_in[2];
  const u16* Awq=(const u16*)d_in[3]; const u16* Awo=(const u16*)d_in[4];
  const u16* ckv=(const u16*)d_in[6]; const u16* Bwq=(const u16*)d_in[7]; const u16* wuk=(const u16*)d_in[8];
  const u16* wuv=(const u16*)d_in[9]; const u16* Bwo=(const u16*)d_in[10];
  float* ws=(float*)d_ws;
  float* xf=ws; float* q=ws+1024; float* qa=q+6144; float* po=qa+H*DC; float* pm=po+256*DC; float* pl=pm+256; float* o=pl+256; float* ctx=o+H*DC;
  float scale=1.0f/sqrtf(192.f);
  k_init<<<3,256,0,stream>>>(Ax,xf);
  for(int r=0;r<2;++r){
    k_lnq<<<96,256,0,stream>>>(xf,Awq,q);
    k_attn<DH><<<256,256,0,stream>>>(q,Kc,Vc,(size_t)SK*DH,scale,po,pm,pl);
    k_comb<DH><<<32,192,0,stream>>>(po,pm,pl,o);
    k_out<<<10,1024,0,stream>>>(o,Awo,xf,nullptr);
    k_lnq<<<96,256,0,stream>>>(xf,Bwq,q);
    k_qabs<<<DC*H/4,256,0,stream>>>(q,wuk,qa);
    k_attn<DC><<<256,256,0,stream>>>(qa,ckv,ckv,0,scale,po,pm,pl);
    k_comb<DC><<<32,256,0,stream>>>(po,pm,pl,o);
    k_ctx<<<96,256,0,stream>>>(o,wuv,ctx);
    k_out<<<10,1024,0,stream>>>(ctx,Bwo,xf,r==1?(u16*)d_out:nullptr);
  }
}
```

```cpp
#include <hip/hip_runtime.h>
#include <hip/hip_bf16.h>
#include <stdint.h>

typedef unsigned short u16;
__device__ __forceinline__ float bf2f(u16 b){ return __builtin_bit_cast(float, (unsigned)b<<16); }
__device__ __forceinline__ u16 f2bf(float f){ unsigned u=__builtin_bit_cast(unsigned,f); u += 0x7fffu + ((u>>16)&1u); return (u16)(u>>16); }
__device__ __forceinline__ float rbf(float f){ return bf2f(f2bf(f)); }

constexpr int DM=640, H=32, DH=192, SK=4096, DC=768;

__device__ float block_sum(float v, float* sh){
  for(int o=32;o;o>>=1) v+=__shfl_xor(v,o);
  int w=threadIdx.x>>6; __syncthreads();
  if((threadIdx.x&63)==0) sh[w]=v; __syncthreads();
  float s=0; for(int i=0;i<(int)(blockDim.x>>6);++i) s+=sh[i];
  return s;
}
__device__ float block_max(float v, float* sh){
  for(int o=32;o;o>>=1) v=fmaxf(v,__shfl_xor(v,o));
  int w=threadIdx.x>>6; __syncthreads();
  if((threadIdx.x&63)==0) sh[w]=v; __syncthreads();
  float s=-INFINITY; for(int i=0;i<(int)(blockDim.x>>6);++i) s=fmaxf(s,sh[i]);
  return s;
}

__global__ void k_init(const u16* x, float* xf){ int i=threadIdx.x+blockIdx.x*blockDim.x; if(i<DM) xf[i]=bf2f(x[i]); }

__global__ void k_lnq(const float* xf, const u16* W, float* q){
  __shared__ float xn[DM]; __shared__ float sh[8]; __shared__ float part[4][64];
  float s=0; for(int i=threadIdx.x;i<DM;i+=256) s+=xf[i];
  float mean=block_sum(s,sh)/DM;
  float v=0; for(int i=threadIdx.x;i<DM;i+=256){ float d=xf[i]-mean; v+=d*d; }
  float var=block_sum(v,sh)/DM;
  float rs=1.0f/sqrtf(var+1e-5f);
  for(int i=threadIdx.x;i<DM;i+=256) xn[i]=rbf((xf[i]-mean)*rs);
  __syncthreads();
  int lane=threadIdx.x&63,w=threadIdx.x>>6; int n=blockIdx.x*64+lane;
  float a=0; for(int m=w*160;m<w*160+160;++m) a+=xn[m]*bf2f(W[(size_t)m*6144+n]);
  part[w][lane]=a; __syncthreads();
  if(w==0) q[n]=rbf(part[0][lane]+part[1][lane]+part[2][lane]+part[3][lane]);
}
template<int L>
__global__ void k_attn(const float* q, const u16* K, const u16* V, size_t hstride, float scale, float* po, float* pm, float* pl){
  __shared__ float qs[L]; __shared__ float p[512]; __shared__ float sh[8];
  int h=blockIdx.x>>3, s=blockIdx.x&7;
  for(int i=threadIdx.x;i<L;i+=256) qs[i]=q[h*L+i];
  __syncthreads();
  const u16* Kh=K+h*hstride+(size_t)s*512*L; const u16* Vh=V+h*hstride+(size_t)s*512*L;
  int lane=threadIdx.x&63,w=threadIdx.x>>6;
  for(int k=w;k<512;k+=4){ float a=0; for(int d=lane;d<L;d+=64) a+=qs[d]*bf2f(Kh[(size_t)k*L+d]);
    for(int o=32;o;o>>=1) a+=__shfl_xor(a,o); if(lane==0) p[k]=a*scale; }
  __syncthreads();
  float m=fmaxf(p[threadIdx.x],p[threadIdx.x+256]); m=block_max(m,sh);
  float e0=__expf(p[threadIdx.x]-m), e1=__expf(p[threadIdx.x+256]-m);
  __syncthreads(); p[threadIdx.x]=e0; p[threadIdx.x+256]=e1;
  float l=block_sum(e0+e1,sh);
  __syncthreads();
  for(int d=threadIdx.x;d<L;d+=256){ float a=0; for(int k=0;k<512;++k) a+=p[k]*bf2f(Vh[(size_t)k*L+d]); po[(size_t)blockIdx.x*L+d]=a; }
  if(threadIdx.x==0){ pm[blockIdx.x]=m; pl[blockIdx.x]=l; }
}
template<int L>
__global__ void k_comb(const float* po,const float* pm,const float* pl,float* o){
  int h=blockIdx.x; float m=-INFINITY; for(int s=0;s<8;++s) m=fmaxf(m,pm[h*8+s]);
  float l=0; for(int s=0;s<8;++s) l+=pl[h*8+s]*__expf(pm[h*8+s]-m);
  for(int d=threadIdx.x;d<L;d+=blockDim.x){ float a=0; for(int s=0;s<8;++s) a+=po[(size_t)(h*8+s)*L+d]*__expf(pm[h*8+s]-m); o[h*L+d]=a/l; }
}
__global__ void k_out(const float* o,const u16* W,float* xf,u16* out){
  __shared__ float part[16][64];
  int lane=threadIdx.x&63,w=threadIdx.x>>6; int n=blockIdx.x*64+lane;
  float a=0; for(int k=w*384;k<w*384+384;++k) a+=rbf(o[k])*bf2f(W[(size_t)k*DM+n]);
  part[w][lane]=a; __syncthreads();
  if(w==0){ float s=0; for(int i=0;i<16;++i) s+=part[i][lane]; float r=rbf(xf[n]+rbf(s)); xf[n]=r; if(out) out[n]=f2bf(r);}
}
__global__ void k_qabs(const float* q,const u16* wuk,float* qa){
  int wv=(blockIdx.x*256+threadIdx.x)>>6, lane=threadIdx.x&63; int c=wv/H,h=wv%H;
  float a=0; for(int d=lane;d<DH;d+=64) a+=q[h*DH+d]*bf2f(wuk[((size_t)c*H+h)*DH+d]);
  for(int o=32;o;o>>=1) a+=__shfl_xor(a,o); if(lane==0) qa[h*DC+c]=a;
}
__global__ void k_ctx(const float* cl,const u16* wuv,float* ctx){
  __shared__ float part[4][64];
  int lane=threadIdx.x&63,w=threadIdx.x>>6; int n=blockIdx.x*64+lane; int h=n/DH;
  float a=0; for(int c=w*192;c<w*192+192;++c) a+=cl[h*DC+c]*bf2f(wuv[(size_t)c*6144+n]);
  part[w][lane]=a; __syncthreads();
  if(w==0) ctx[n]=part[0][lane]+part[1][lane]+part[2][lane]+part[3][lane];
}

#define MASK 508

typedef unsigned v4u __attribute__((ext_vector_type(4)));
template<int AUX>
__device__ __forceinline__ unsigned stream_one(const void* p, unsigned bytes){
  __amdgpu_buffer_rsrc_t rs = __builtin_amdgcn_make_buffer_rsrc((void*)p, 0, bytes, 0x00020000);
  unsigned per = bytes/256; unsigned base=blockIdx.x*per + threadIdx.x*16; unsigned acc=0;
  for (unsigned o=0;o<per;o+=4096*6){
    v4u v[6];
    #pragma unroll
    for(int i=0;i<6;++i) v[i]=__builtin_amdgcn_raw_buffer_load_b128(rs, (o+i*4096<per)? base+o+i*4096 : 0xfffffff0u, 0, AUX);
    #pragma unroll
    for(int i=0;i<6;++i) acc+=v[i].x^v[i].y^v[i].z^v[i].w;
  }
  return acc;
}
struct Ptrs { const void* p[10]; };
template<int AUX>
__global__ void __launch_bounds__(1024) k_probe(Ptrs P, unsigned long long* ts, unsigned* sink){
  unsigned long long t0=__builtin_amdgcn_s_memrealtime();
  unsigned acc=0;
  const unsigned sz[10]={50331648u,50331648u,7864320u,7864320u,6291456u,7864320u,9437184u,9437184u,7864320u,0u};
  #pragma unroll
  for(int t=0;t<9;++t) if ((MASK>>t)&1){
    __amdgpu_buffer_rsrc_t rs = __builtin_amdgcn_make_buffer_rsrc((void*)P.p[t], 0, sz[t], 0x00020000);
    unsigned per = sz[t]/256; unsigned base=blockIdx.x*per;
    for (unsigned o=threadIdx.x*16;o<per;o+=16384*4){
      v4u v[4];
      #pragma unroll
      for(int i=0;i<4;++i) v[i]=__builtin_amdgcn_raw_buffer_load_b128(rs, (o+i*16384<per)? base+o+i*16384 : 0xfffffff0u, 0, AUX);
      #pragma unroll
      for(int i=0;i<4;++i) acc+=v[i].x^v[i].y^v[i].z^v[i].w;
    }
  }
  sink[blockIdx.x*1024+threadIdx.x]=acc;
  __syncthreads();
  if(threadIdx.x==0){ ts[blockIdx.x*2]=t0; ts[blockIdx.x*2+1]=__builtin_amdgcn_s_memrealtime(); }
}
__global__ void k_spin(const unsigned long long* ts){
  unsigned long long mn=~0ull,mx=0; for(int b=0;b<256;++b){ mn=min(mn,ts[2*b]); mx=max(mx,ts[2*b+1]); }
  unsigned long long d=(mx-mn)*100ull; if (d>2000000ull) d=2000000ull;
  unsigned long long t=__builtin_amdgcn_s_memrealtime(); while(__builtin_amdgcn_s_memrealtime()-t<d) __builtin_amdgcn_s_sleep(4);
}
extern "C" void kernel_launch(void* const* d_in, const int* in_sizes, int n_in,
                              void* d_out, int out_size, void* d_ws, size_t ws_size,
                              hipStream_t stream) {
  const u16* Ax=(const u16*)d_in[0]; const u16* Kc=(const u16*)d_in[1]; const u16* Vc=(const u16*)d_in[2];
  const u16* Awq=(const u16*)d_in[3]; const u16* Awo=(const u16*)d_in[4];
  const u16* ckv=(const u16*)d_in[6]; const u16* Bwq=(const u16*)d_in[7]; const u16* wuk=(const u16*)d_in[8];
  const u16* wuv=(const u16*)d_in[9]; const u16* Bwo=(const u16*)d_in[10];
  float* ws=(float*)d_ws;
  float* xf=ws; float* q=ws+1024; float* qa=q+6144; float* po=qa+H*DC; float* pm=po+256*DC; float* pl=pm+256; float* o=pl+256; float* ctx=o+H*DC;
  float scale=1.0f/sqrtf(192.f);
  Ptrs P; P.p[0]=Kc;P.p[1]=Vc;P.p[2]=Awq;P.p[3]=Awo;P.p[4]=ckv;P.p[5]=Bwq;P.p[6]=wuk;P.p[7]=wuv;P.p[8]=Bwo;P.p[9]=Bwo;
  unsigned long long* ts=(unsigned long long*)((char*)d_ws+(64u<<20)); unsigned* sink=(unsigned*)((char*)d_ws+(65u<<20));
  k_probe<0><<<256,1024,0,stream>>>(P,ts,sink);
  k_spin<<<1,64,0,stream>>>(ts);
  k_init<<<3,256,0,stream>>>(Ax,xf);
  for(int r=0;r<2;++r){
    k_lnq<<<96,256,0,stream>>>(xf,Awq,q);
    k_attn<DH><<<256,256,0,stream>>>(q,Kc,Vc,(size_t)SK*DH,scale,po,pm,pl);
    k_comb<DH><<<32,192,0,stream>>>(po,pm,pl,o);
    k_out<<<10,1024,0,stream>>>(o,Awo,xf,nullptr);
    k_lnq<<<96,256,0,stream>>>(xf,Bwq,q);
    k_qabs<<<DC*H/4,256,0,stream>>>(q,wuk,qa);
    k_attn<DC><<<256,256,0,stream>>>(qa,ckv,ckv,0,scale,po,pm,pl);
    k_comb<DC><<<32,256,0,stream>>>(po,pm,pl,o);
    k_ctx<<<96,256,0,stream>>>(o,wuv,ctx);
    k_out<<<10,1024,0,stream>>>(ctx,Bwo,xf,r==1?(u16*)d_out:nullptr);
  }
}
```

```cpp
#include <hip/hip_runtime.h>
#include <stdint.h>
struct Args { const void* p[12]; };
__global__ void __launch_bounds__(256, 1) decode_step(Args args) {
  unsigned tid = threadIdx.x, bid = blockIdx.x;
  unsigned long long ka = (unsigned long long)__builtin_amdgcn_kernarg_segment_ptr();
  asm volatile(
"s_mov_b32 s65, 20000\n"
".Lcode0_%=:\n"
"s_memrealtime s[68:69]\n"
"s_load_dwordx16 s[84:99], s[0:1], 0\n"
"s_load_dwordx8 s[56:63], s[0:1], 64\n"
"v_lshrrev_b32 v1, 6, v0\n"
"s_nop 0\n"
"v_readfirstlane_b32 s48, v1\n"
"v_and_b32 v0, 63, v0\n"
"s_and_b32 s77, s2, 7\n"
"s_lshr_b32 s78, s2, 3\n"
"s_lshr_b32 s79, s78, 3\n"
"s_lshl2_add_u32 s49, s77, s79\n"
"s_and_b32 s50, s78, 7\n"
"s_lshr_b32 s51, s77, 2\n"
"s_and_b32 s52, s78, 1\n"
"s_and_b32 s77, s77, 3\n"
"s_lshr_b32 s78, s78, 1\n"
"s_lshl4_add_u32 s53, s77, s78\n"
"s_mul_i32 s75, s48, 12288\n"
"s_lshl3_add_u32 s64, s49, s50\n"
"s_lshl2_add_u32 s64, s64, s48\n"
"s_mul_i32 s64, s64, 49152\n"
"s_mul_i32 s79, s49, 384\n"
"s_mul_i32 s67, s50, 983040\n"
"s_add_u32 s67, s67, s79\n"
"v_lshrrev_b32 v1, 3, v0\n"
"v_add_u32 v1, 0, v1\n"
"v_mul_u32_u24 v2, 43, v1\n"
"v_lshrrev_b32 v2, 7, v2\n"
"v_mad_i32_i24 v1, v2, -3, v1\n"
"v_lshrrev_b32 v44, 1, v2\n"
"v_xor_b32 v44, v0, v44\n"
"v_and_b32 v44, 7, v44\n"
"v_lshl_add_u32 v44, v1, 3, v44\n"
"v_lshlrev_b32 v44, 4, v44\n"
"v_mul_u32_u24 v2, 384, v2\n"
"v_add_u32 v44, v44, v2\n"
"v_lshrrev_b32 v1, 3, v0\n"
"v_add_u32 v1, 8, v1\n"
"v_mul_u32_u24 v2, 43, v1\n"
"v_lshrrev_b32 v2, 7, v2\n"
"v_mad_i32_i24 v1, v2, -3, v1\n"
"v_lshrrev_b32 v45, 1, v2\n"
"v_xor_b32 v45, v0, v45\n"
"v_and_b32 v45, 7, v45\n"
"v_lshl_add_u32 v45, v1, 3, v45\n"
"v_lshlrev_b32 v45, 4, v45\n"
"v_mul_u32_u24 v2, 384, v2\n"
"v_add_u32 v45, v45, v2\n"
"v_subrev_u32 v45, 1024, v45\n"
"v_lshrrev_b32 v1, 3, v0\n"
"v_add_u32 v1, 16, v1\n"
"v_mul_u32_u24 v2, 43, v1\n"
"v_lshrrev_b32 v2, 7, v2\n"
"v_mad_i32_i24 v1, v2, -3, v1\n"
"v_lshrrev_b32 v46, 1, v2\n"
"v_xor_b32 v46, v0, v46\n"
"v_and_b32 v46, 7, v46\n"
"v_lshl_add_u32 v46, v1, 3, v46\n"
"v_lshlrev_b32 v46, 4, v46\n"
"v_mul_u32_u24 v2, 384, v2\n"
"v_add_u32 v46, v46, v2\n"
"v_subrev_u32 v46, 2048, v46\n"
"v_lshrrev_b32 v1, 3, v0\n"
"v_add_u32 v1, 24, v1\n"
"v_mul_u32_u24 v2, 43, v1\n"
"v_lshrrev_b32 v2, 7, v2\n"
"v_mad_i32_i24 v1, v2, -3, v1\n"
"v_lshrrev_b32 v47, 1, v2\n"
"v_xor_b32 v47, v0, v47\n"
"v_and_b32 v47, 7, v47\n"
"v_lshl_add_u32 v47, v1, 3, v47\n"
"v_lshlrev_b32 v47, 4, v47\n"
"v_mul_u32_u24 v2, 384, v2\n"
"v_add_u32 v47, v47, v2\n"
"v_lshrrev_b32 v1, 3, v0\n"
"v_add_u32 v1, 32, v1\n"
"v_mul_u32_u24 v2, 43, v1\n"
"v_lshrrev_b32 v2, 7, v2\n"
"v_mad_i32_i24 v1, v2, -3, v1\n"
"v_lshrrev_b32 v48, 1, v2\n"
"v_xor_b32 v48, v0, v48\n"
"v_and_b32 v48, 7, v48\n"
"v_lshl_add_u32 v48, v1, 3, v48\n"
"v_lshlrev_b32 v48, 4, v48\n"
"v_mul_u32_u24 v2, 384, v2\n"
"v_add_u32 v48, v48, v2\n"
"v_subrev_u32 v48, 1024, v48\n"
"v_lshrrev_b32 v1, 3, v0\n"
"v_add_u32 v1, 40, v1\n"
"v_mul_u32_u24 v2, 43, v1\n"
"v_lshrrev_b32 v2, 7, v2\n"
"v_mad_i32_i24 v1, v2, -3, v1\n"
"v_lshrrev_b32 v49, 1, v2\n"
"v_xor_b32 v49, v0, v49\n"
"v_and_b32 v49, 7, v49\n"
"v_lshl_add_u32 v49, v1, 3, v49\n"
"v_lshlrev_b32 v49, 4, v49\n"
"v_mul_u32_u24 v2, 384, v2\n"
"v_add_u32 v49, v49, v2\n"
"v_subrev_u32 v49, 2048, v49\n"
"s_waitcnt lgkmcnt(0)\n"
"s_mov_b32 s3, s68\n"
"s_add_u32 s4, s86, s64\n"
"s_addc_u32 s5, s87, 0\n"
"s_mov_b32 s6, 49152\n"
"s_mov_b32 s7, 0x20000\n"
"s_add_u32 m0, s75, 114688\n"
"s_mov_b32 s76, 0\n"
"buffer_load_dwordx4 v44, s[4:7], s76 offen offset:0 nt lds\n"
"buffer_load_dwordx4 v45, s[4:7], s76 offen offset:1024 nt lds\n"
"buffer_load_dwordx4 v46, s[4:7], s76 offen offset:2048 nt lds\n"
"s_add_u32 m0, m0, 3072\n"
"s_nop 0\n"
"buffer_load_dwordx4 v47, s[4:7], s76 offen offset:0 nt lds\n"
"buffer_load_dwordx4 v48, s[4:7], s76 offen offset:1024 nt lds\n"
"buffer_load_dwordx4 v49, s[4:7], s76 offen offset:2048 nt lds\n"
"s_add_u32 s62, s62, 0x3fffff\n"
"s_addc_u32 s63, s63, 0\n"
"s_andn2_b32 s62, s62, 0x3fffff\n"
"s_mov_b32 s40, s62\n"
"s_mov_b32 s41, s63\n"
"s_mov_b32 s42, 6291456\n"
"s_mov_b32 s43, 0x20000\n"
"s_add_u32 s12, s90, s67\n"
"s_addc_u32 s13, s91, 0\n"
"s_mov_b32 s14, 971136\n"
"s_mov_b32 s15, 0x20000\n"
"s_mov_b32 s77, 5390336\n"
"buffer_load_dword v43, off, s[40:43], s77 sc1\n"
"s_cmp_lg_u32 s48, 0\n"
"s_cbranch_scc0 .Lr_1_%=\n"
"s_lshl_b32 s70, s48, 4\n"
"s_sub_u32 s70, s70, 16\n"
"v_lshrrev_b32 v3, 3, v0\n"
"v_add_u32 v3, s70, v3\n"
"v_mul_u32_u24 v4, 43, v3\n"
"v_lshrrev_b32 v4, 7, v4\n"
"v_mad_i32_i24 v3, v4, -3, v3\n"
"v_lshrrev_b32 v1, 1, v4\n"
"v_and_b32 v1, 4, v1\n"
"v_and_or_b32 v1, v4, 2, v1\n"
"v_xor_b32 v1, v0, v1\n"
"v_and_b32 v1, 7, v1\n"
"v_lshl_add_u32 v1, v3, 3, v1\n"
"v_lshlrev_b32 v1, 4, v1\n"
"v_mul_u32_u24 v4, 12288, v4\n"
"v_add_u32 v1, v1, v4\n"
"s_add_u32 s71, s70, 8\n"
"v_lshrrev_b32 v3, 3, v0\n"
"v_add_u32 v3, s71, v3\n"
"v_mul_u32_u24 v4, 43, v3\n"
"v_lshrrev_b32 v4, 7, v4\n"
"v_mad_i32_i24 v3, v4, -3, v3\n"
"v_lshrrev_b32 v2, 1, v4\n"
"v_and_b32 v2, 4, v2\n"
"v_and_or_b32 v2, v4, 2, v2\n"
"v_xor_b32 v2, v0, v2\n"
"v_and_b32 v2, 7, v2\n"
"v_lshl_add_u32 v2, v3, 3, v2\n"
"v_lshlrev_b32 v2, 4, v2\n"
"v_mul_u32_u24 v4, 12288, v4\n"
"v_add_u32 v2, v2, v4\n"
"s_lshl_b32 s70, s70, 7\n"
"s_add_u32 m0, s70, 10240\n"
"s_mov_b32 s71, 0\n"
"buffer_load_dwordx4 v1, s[12:15], s71 offen offset:0 nt lds\n"
"s_add_u32 m0, m0, 1024\n"
"s_nop 0\n"
"buffer_load_dwordx4 v2, s[12:15], s71 offen offset:0 nt lds\n"
"s_add_u32 m0, s70, 16384\n"
"s_mov_b32 s71, 196608\n"
"buffer_load_dwordx4 v1, s[12:15], s71 offen offset:0 nt lds\n"
"s_add_u32 m0, m0, 1024\n"
"s_nop 0\n"
"buffer_load_dwordx4 v2, s[12:15], s71 offen offset:0 nt lds\n"
"s_add_u32 m0, s70, 22528\n"
"s_mov_b32 s71, 393216\n"
"buffer_load_dwordx4 v1, s[12:15], s71 offen offset:0 nt lds\n"
"s_add_u32 m0, m0, 1024\n"
"s_nop 0\n"
"buffer_load_dwordx4 v2, s[12:15], s71 offen offset:0 nt lds\n"
"s_add_u32 m0, s70, 28672\n"
"s_mov_b32 s71, 589824\n"
"buffer_load_dwordx4 v1, s[12:15], s71 offen offset:0 nt lds\n"
"s_add_u32 m0, m0, 1024\n"
"s_nop 0\n"
"buffer_load_dwordx4 v2, s[12:15], s71 offen offset:0 nt lds\n"
"s_add_u32 m0, s70, 34816\n"
"s_mov_b32 s71, 786432\n"
"buffer_load_dwordx4 v1, s[12:15], s71 offen offset:0 nt lds\n"
"s_add_u32 m0, m0, 1024\n"
"s_nop 0\n"
"buffer_load_dwordx4 v2, s[12:15], s71 offen offset:0 nt lds\n"
".Lr_1_%=:\n"
"s_cmp_eq_u32 s48, 0\n"
"s_cbranch_scc0 .Lr_2_%=\n"
"s_mov_b32 s44, s84\n"
"s_mov_b32 s45, s85\n"
"s_mov_b32 s46, 1280\n"
"s_mov_b32 s47, 0x20000\n"
"v_lshlrev_b32 v1, 4, v0\n"
"v_lshlrev_b32 v2, 2, v0\n"
"buffer_load_dwordx4 v[8:11], v1, s[44:47], 0 offen\n"
"buffer_load_dword v12, v2, s[44:47], 0 offen offset:1024\n"
"v_and_b32 v3, 15, v0\n"
"v_lshrrev_b32 v4, 4, v0\n"
"v_lshl_add_u32 v3, v3, 1, v4\n"
"v_lshlrev_b32 v3, 3, v3\n"
"s_mul_i32 s77, s50, 160\n"
"buffer_load_dwordx2 v[14:15], v3, s[44:47], s77 offen\n"
"v_mov_b64 v[16:17], -1\n"
"v_mov_b64 v[18:19], -1\n"
"v_and_b32 v1, 15, v0\n"
"v_lshrrev_b32 v2, 4, v0\n"
"v_min_u32 v4, 9, v1\n"
"v_lshlrev_b32 v4, 4, v4\n"
"v_lshl_add_u32 v4, v2, 17, v4\n"
"s_mul_i32 s77, s49, 160\n"
"s_lshl_b32 s78, s50, 14\n"
"s_add_u32 s77, s77, s78\n"
"s_add_u32 s77, s77, 4718592\n"
"buffer_store_dwordx4 v[16:19], v4, s[40:43], s77 offen offset:0 sc1\n"
"s_add_u32 s77, s77, 8192\n"
"buffer_store_dwordx4 v[16:19], v4, s[40:43], s77 offen offset:0 sc1\n"
"v_min_u32 v4, 11, v1\n"
"v_lshlrev_b32 v4, 4, v4\n"
"v_and_b32 v2, 1, v2\n"
"v_lshl_add_u32 v4, v2, 16, v4\n"
"s_bfe_u32 s77, s49, 0x10003\n"
"s_lshr_b32 s78, s50, 2\n"
"s_lshl1_add_u32 s77, s77, s78\n"
"s_lshl_b32 s77, s77, 14\n"
"s_and_b32 s78, s49, 7\n"
"s_mul_i32 s78, s78, 768\n"
"s_add_u32 s77, s77, s78\n"
"s_and_b32 s78, s50, 3\n"
"s_mul_i32 s78, s78, 192\n"
"s_add_u32 s77, s77, s78\n"
"s_add_u32 s77, s77, 5242880\n"
"s_lshl_b32 s78, s51, 13\n"
"s_add_u32 s77, s77, s78\n"
"buffer_store_dwordx4 v[16:19], v4, s[40:43], s77 offen offset:0 sc1\n"
".Lr_2_%=:\n"
"s_add_u32 m0, s75, 120832\n"
"s_mov_b32 s76, 6144\n"
"buffer_load_dwordx4 v44, s[4:7], s76 offen offset:0 nt lds\n"
"buffer_load_dwordx4 v45, s[4:7], s76 offen offset:1024 nt lds\n"
"buffer_load_dwordx4 v46, s[4:7], s76 offen offset:2048 nt lds\n"
"s_add_u32 m0, m0, 3072\n"
"s_nop 0\n"
"buffer_load_dwordx4 v47, s[4:7], s76 offen offset:0 nt lds\n"
"buffer_load_dwordx4 v48, s[4:7], s76 offen offset:1024 nt lds\n"
"buffer_load_dwordx4 v49, s[4:7], s76 offen offset:2048 nt lds\n"
"s_add_u32 s8, s88, s64\n"
"s_addc_u32 s9, s89, 0\n"
"s_mov_b32 s10, 49152\n"
"s_mov_b32 s11, 0x20000\n"
"s_add_u32 s20, s96, s67\n"
"s_addc_u32 s21, s97, 0\n"
"s_mov_b32 s22, 971136\n"
"s_mov_b32 s23, 0x20000\n"
"s_mul_i32 s67, s50, 1179648\n"
"s_add_u32 s67, s67, s79\n"
"s_add_u32 s24, s98, s67\n"
"s_addc_u32 s25, s99, 0\n"
"s_mov_b32 s26, 1167744\n"
"s_mov_b32 s27, 0x20000\n"
"s_add_u32 s32, s56, s67\n"
"s_addc_u32 s33, s57, 0\n"
"s_mov_b32 s34, 1167744\n"
"s_mov_b32 s35, 0x20000\n"
"s_mul_i32 s67, s49, 245760\n"
"s_mul_i32 s64, s50, 160\n"
"s_add_u32 s67, s67, s64\n"
"s_add_u32 s16, s92, s67\n"
"s_addc_u32 s17, s93, 0\n"
"s_mov_b32 s18, 245760\n"
"s_mov_b32 s19, 0x20000\n"
"s_add_u32 s36, s58, s67\n"
"s_addc_u32 s37, s59, 0\n"
"s_mov_b32 s38, 245760\n"
"s_mov_b32 s39, 0x20000\n"
"s_sub_u32 s18, s18, s64\n"
"s_mov_b32 s38, s18\n"
"s_mul_i32 s67, s53, 98304\n"
"s_add_u32 s28, s94, s67\n"
"s_addc_u32 s29, s95, 0\n"
"s_mov_b32 s30, 98304\n"
"s_mov_b32 s31, 0x20000\n"
"s_mov_b64 s[68:69], s[60:61]\n"
"s_cmp_eq_u32 s48, 3\n"
"s_cbranch_scc0 .Lr_3_%=\n"
"s_getpc_b64 s[44:45]\n"
".Lpc_%=:\n"
"s_mov_b32 s46, .Lcode1_%=-.Lpc_%=\n"
"s_mov_b32 s47, 0x20000\n"
".Lr_3_%=:\n"
"s_cmp_eq_u32 s48, 3\n"
"s_cbranch_scc0 .Lr_4_%=\n"
"s_lshr_b32 s70, s2, 3\n"
"s_lshl_b32 s70, s70, 11\n"
"v_lshlrev_b32 v1, 7, v0\n"
"s_mov_b32 m0, 3968\n"
"s_mov_b64 exec, 0xffff\n"
"buffer_load_dword v1, s[44:47], s70 offen lds\n"
"s_mov_b64 exec, -1\n"
".Lr_4_%=:\n"
"v_lshrrev_b32 v1, 3, v0\n"
"v_add_u32 v1, 0, v1\n"
"v_mul_u32_u24 v2, 43, v1\n"
"v_lshrrev_b32 v2, 7, v2\n"
"v_mad_i32_i24 v1, v2, -3, v1\n"
"v_lshrrev_b32 v50, 1, v2\n"
"v_and_b32 v50, 4, v50\n"
"v_and_or_b32 v50, v2, 2, v50\n"
"v_xor_b32 v50, v0, v50\n"
"v_and_b32 v50, 7, v50\n"
"v_lshl_add_u32 v50, v1, 3, v50\n"
"v_lshlrev_b32 v50, 4, v50\n"
"v_mul_u32_u24 v2, 384, v2\n"
"v_add_u32 v50, v50, v2\n"
"v_lshrrev_b32 v1, 3, v0\n"
"v_add_u32 v1, 8, v1\n"
"v_mul_u32_u24 v2, 43, v1\n"
"v_lshrrev_b32 v2, 7, v2\n"
"v_mad_i32_i24 v1, v2, -3, v1\n"
"v_lshrrev_b32 v51, 1, v2\n"
"v_and_b32 v51, 4, v51\n"
"v_and_or_b32 v51, v2, 2, v51\n"
"v_xor_b32 v51, v0, v51\n"
"v_and_b32 v51, 7, v51\n"
"v_lshl_add_u32 v51, v1, 3, v51\n"
"v_lshlrev_b32 v51, 4, v51\n"
"v_mul_u32_u24 v2, 384, v2\n"
"v_add_u32 v51, v51, v2\n"
"v_subrev_u32 v51, 1024, v51\n"
"v_lshrrev_b32 v1, 3, v0\n"
"v_add_u32 v1, 16, v1\n"
"v_mul_u32_u24 v2, 43, v1\n"
"v_lshrrev_b32 v2, 7, v2\n"
"v_mad_i32_i24 v1, v2, -3, v1\n"
"v_lshrrev_b32 v52, 1, v2\n"
"v_and_b32 v52, 4, v52\n"
"v_and_or_b32 v52, v2, 2, v52\n"
"v_xor_b32 v52, v0, v52\n"
"v_and_b32 v52, 7, v52\n"
"v_lshl_add_u32 v52, v1, 3, v52\n"
"v_lshlrev_b32 v52, 4, v52\n"
"v_mul_u32_u24 v2, 384, v2\n"
"v_add_u32 v52, v52, v2\n"
"v_subrev_u32 v52, 2048, v52\n"
"v_lshrrev_b32 v1, 3, v0\n"
"v_add_u32 v1, 24, v1\n"
"v_mul_u32_u24 v2, 43, v1\n"
"v_lshrrev_b32 v2, 7, v2\n"
"v_mad_i32_i24 v1, v2, -3, v1\n"
"v_lshrrev_b32 v53, 1, v2\n"
"v_and_b32 v53, 4, v53\n"
"v_and_or_b32 v53, v2, 2, v53\n"
"v_xor_b32 v53, v0, v53\n"
"v_and_b32 v53, 7, v53\n"
"v_lshl_add_u32 v53, v1, 3, v53\n"
"v_lshlrev_b32 v53, 4, v53\n"
"v_mul_u32_u24 v2, 384, v2\n"
"v_add_u32 v53, v53, v2\n"
"v_lshrrev_b32 v1, 3, v0\n"
"v_add_u32 v1, 32, v1\n"
"v_mul_u32_u24 v2, 43, v1\n"
"v_lshrrev_b32 v2, 7, v2\n"
"v_mad_i32_i24 v1, v2, -3, v1\n"
"v_lshrrev_b32 v54, 1, v2\n"
"v_and_b32 v54, 4, v54\n"
"v_and_or_b32 v54, v2, 2, v54\n"
"v_xor_b32 v54, v0, v54\n"
"v_and_b32 v54, 7, v54\n"
"v_lshl_add_u32 v54, v1, 3, v54\n"
"v_lshlrev_b32 v54, 4, v54\n"
"v_mul_u32_u24 v2, 384, v2\n"
"v_add_u32 v54, v54, v2\n"
"v_subrev_u32 v54, 1024, v54\n"
"v_lshrrev_b32 v1, 3, v0\n"
"v_add_u32 v1, 40, v1\n"
"v_mul_u32_u24 v2, 43, v1\n"
"v_lshrrev_b32 v2, 7, v2\n"
"v_mad_i32_i24 v1, v2, -3, v1\n"
"v_lshrrev_b32 v55, 1, v2\n"
"v_and_b32 v55, 4, v55\n"
"v_and_or_b32 v55, v2, 2, v55\n"
"v_xor_b32 v55, v0, v55\n"
"v_and_b32 v55, 7, v55\n"
"v_lshl_add_u32 v55, v1, 3, v55\n"
"v_lshlrev_b32 v55, 4, v55\n"
"v_mul_u32_u24 v2, 384, v2\n"
"v_add_u32 v55, v55, v2\n"
"v_subrev_u32 v55, 2048, v55\n"
"s_add_u32 s77, s75, 65536\n"
"v_and_b32 v1, 15, v0\n"
"v_lshrrev_b32 v56, 1, v1\n"
"v_lshrrev_b32 v57, 4, v0\n"
"v_xor_b32 v56, v56, v57\n"
"v_mul_u32_u24 v1, 24, v1\n"
"v_add_lshl_u32 v56, v56, v1, 4\n"
"v_add_u32 v56, s77, v56\n"
"v_xor_b32 v57, 64, v56\n"
"v_lshrrev_b32 v1, 1, v0\n"
"v_and_b32 v1, 16, v1\n"
"v_bfe_u32 v2, v0, 2, 2\n"
"v_or_b32 v1, v1, v2\n"
"v_mul_u32_u24 v1, 384, v1\n"
"v_and_b32 v4, 3, v0\n"
"v_lshl_add_u32 v1, v4, 3, v1\n"
"v_add_u32 v1, s77, v1\n"
"v_and_b32 v2, 2, v2\n"
"v_lshrrev_b32 v4, 3, v0\n"
"v_and_b32 v4, 2, v4\n"
"v_xor_b32 v5, 0, v4\n"
"v_and_b32 v7, 2, v5\n"
"v_lshl_or_b32 v7, v7, 1, v2\n"
"v_xor_b32 v7, 0, v7\n"
"v_lshl_add_u32 v58, v7, 4, v1\n"
"v_mul_u32_u24 v5, 1536, v5\n"
"v_add_u32 v58, v58, v5\n"
"v_xor_b32 v5, 3, v4\n"
"v_and_b32 v7, 2, v5\n"
"v_lshl_or_b32 v7, v7, 1, v2\n"
"v_xor_b32 v7, 0, v7\n"
"v_lshl_add_u32 v59, v7, 4, v1\n"
"v_mul_u32_u24 v5, 1536, v5\n"
"v_add_u32 v59, v59, v5\n"
"v_xor_b32 v5, 1, v4\n"
"v_and_b32 v7, 2, v5\n"
"v_lshl_or_b32 v7, v7, 1, v2\n"
"v_xor_b32 v7, 2, v7\n"
"v_lshl_add_u32 v60, v7, 4, v1\n"
"v_mul_u32_u24 v5, 1536, v5\n"
"v_add_u32 v60, v60, v5\n"
"v_xor_b32 v5, 0, v4\n"
"v_and_b32 v7, 2, v5\n"
"v_lshl_or_b32 v7, v7, 1, v2\n"
"v_xor_b32 v7, 2, v7\n"
"v_lshl_add_u32 v61, v7, 4, v1\n"
"v_mul_u32_u24 v5, 1536, v5\n"
"v_add_u32 v61, v61, v5\n"
"v_xor_b32 v5, 2, v4\n"
"v_and_b32 v7, 2, v5\n"
"v_lshl_or_b32 v7, v7, 1, v2\n"
"v_xor_b32 v7, 4, v7\n"
"v_lshl_add_u32 v62, v7, 4, v1\n"
"v_mul_u32_u24 v5, 1536, v5\n"
"v_add_u32 v62, v62, v5\n"
"v_xor_b32 v5, 1, v4\n"
"v_and_b32 v7, 2, v5\n"
"v_lshl_or_b32 v7, v7, 1, v2\n"
"v_xor_b32 v7, 4, v7\n"
"v_lshl_add_u32 v63, v7, 4, v1\n"
"v_mul_u32_u24 v5, 1536, v5\n"
"v_add_u32 v63, v63, v5\n"
"v_xor_b32 v5, 3, v4\n"
"v_and_b32 v7, 2, v5\n"
"v_lshl_or_b32 v7, v7, 1, v2\n"
"v_xor_b32 v7, 6, v7\n"
"v_lshl_add_u32 v64, v7, 4, v1\n"
"v_mul_u32_u24 v5, 1536, v5\n"
"v_add_u32 v64, v64, v5\n"
"v_xor_b32 v5, 2, v4\n"
"v_and_b32 v7, 2, v5\n"
"v_lshl_or_b32 v7, v7, 1, v2\n"
"v_xor_b32 v7, 6, v7\n"
"v_lshl_add_u32 v65, v7, 4, v1\n"
"v_mul_u32_u24 v5, 1536, v5\n"
"v_add_u32 v65, v65, v5\n"
"v_and_b32 v6, 1, v0\n"
"v_mul_u32_u24 v6, 0xaaaa, v6\n"
"v_add_u32 v6, 0x4444, v6\n"
"s_waitcnt vmcnt(17)\n"
"s_cmp_lg_u32 s48, 3\n"
"s_cbranch_scc0 .Lr_5_%=\n"
"s_waitcnt vmcnt(16)\n"
".Lr_5_%=:\n"
"s_cmp_eq_u32 s48, 0\n"
"s_cbranch_scc0 .Lr_6_%=\n"
"s_waitcnt vmcnt(12)\n"
".Lr_6_%=:\n"
"v_readfirstlane_b32 s54, v43\n"
"s_cmp_eq_u32 s48, 0\n"
"s_cbranch_scc0 .Lr_7_%=\n"
"s_and_b32 s77, s2, 15\n"
"s_lshl_b32 s77, s77, 14\n"
"s_lshr_b32 s78, s2, 4\n"
"s_lshl_b32 s78, s78, 9\n"
"s_add_u32 s77, s77, s78\n"
"s_bfe_u32 s67, s2, 0x10003\n"
"s_lshl_b32 s78, s67, 13\n"
"s_add_u32 s77, s77, s78\n"
"s_add_u32 s77, s77, 5505024\n"
"s_xor_b32 s78, s77, 8192\n"
"s_add_u32 s79, s77, 256\n"
"s_buffer_load_dword s64, s[40:43], s79\n"
"s_waitcnt lgkmcnt(0)\n"
"s_memtime s[56:57]\n"
"s_waitcnt lgkmcnt(0)\n"
"s_buffer_load_dword s64, s[40:43], s77\n"
"s_waitcnt lgkmcnt(0)\n"
"s_memtime s[58:59]\n"
"s_waitcnt lgkmcnt(0)\n"
"s_buffer_load_dword s64, s[40:43], s78\n"
"s_waitcnt lgkmcnt(0)\n"
"s_memtime s[60:61]\n"
"s_waitcnt lgkmcnt(0)\n"
"s_sub_u32 s56, s58, s56\n"
"s_sub_u32 s58, s60, s58\n"
"s_cmp_lt_u32 s58, s56\n"
"s_cselect_b32 s64, 1, 0\n"
"s_xor_b32 s64, s64, s67\n"
"s_xor_b32 s64, s64, s51\n"
"s_waitcnt vmcnt(6)\n"
"s_add_u32 s66, s54, 11\n"
"s_lshl1_add_u32 s64, s3, s64\n"
"v_mov_b32 v16, s64\n"
"v_mov_b32 v17, s66\n"
"s_lshl_b32 s77, s2, 3\n"
"s_add_u32 s77, s77, 5373952\n"
"s_mov_b64 exec, 1\n"
"buffer_store_dwordx2 v[16:17], off, s[40:43], s77 sc1\n"
"s_mov_b64 exec, -1\n"
"v_mov_b32 v13, 0\n"
"v_lshlrev_b32 v1, 16, v8\n"
"v_and_b32 v2, 0xffff0000, v8\n"
"v_add_f32 v13, v13, v1\n"
"v_add_f32 v13, v13, v2\n"
"v_lshlrev_b32 v1, 16, v9\n"
"v_and_b32 v2, 0xffff0000, v9\n"
"v_add_f32 v13, v13, v1\n"
"v_add_f32 v13, v13, v2\n"
"v_lshlrev_b32 v1, 16, v10\n"
"v_and_b32 v2, 0xffff0000, v10\n"
"v_add_f32 v13, v13, v1\n"
"v_add_f32 v13, v13, v2\n"
"v_lshlrev_b32 v1, 16, v11\n"
"v_and_b32 v2, 0xffff0000, v11\n"
"v_add_f32 v13, v13, v1\n"
"v_add_f32 v13, v13, v2\n"
"v_lshlrev_b32 v1, 16, v12\n"
"v_and_b32 v2, 0xffff0000, v12\n"
"v_add_f32 v13, v13, v1\n"
"v_add_f32 v13, v13, v2\n"
"s_nop 1\n"
"v_add_f32_dpp v13, v13, v13 row_ror:1 row_mask:0xf bank_mask:0xf\n"
"s_nop 1\n"
"v_add_f32_dpp v13, v13, v13 row_ror:2 row_mask:0xf bank_mask:0xf\n"
"s_nop 1\n"
"v_add_f32_dpp v13, v13, v13 row_ror:4 row_mask:0xf bank_mask:0xf\n"
"s_nop 1\n"
"v_add_f32_dpp v13, v13, v13 row_ror:8 row_mask:0xf bank_mask:0xf\n"
"v_mov_b32 v1, v13\n"
"s_nop 1\n"
"v_permlane16_swap_b32 v13, v1\n"
"v_add_f32 v13, v13, v1\n"
"v_mov_b32 v1, v13\n"
"s_nop 1\n"
"v_permlane32_swap_b32 v13, v1\n"
"v_add_f32 v13, v13, v1\n"
"v_mul_f32 v13, 0x3acccccd, v13\n"
"s_nop 0\n"
"v_readfirstlane_b32 s55, v13\n"
"v_lshlrev_b32 v40, 16, v14\n"
"v_and_b32 v41, 0xffff0000, v14\n"
"v_lshlrev_b32 v42, 16, v15\n"
"v_and_b32 v43, 0xffff0000, v15\n"
"v_sub_f32 v40, v40, v13\n"
"v_sub_f32 v41, v41, v13\n"
"v_sub_f32 v42, v42, v13\n"
"v_sub_f32 v43, v43, v13\n"
"v_lshlrev_b32 v2, 2, v0\n"
"v_mov_b32 v1, 0x3f803f80\n"
"s_mov_b32 exec_hi, 0xff\n"
"ds_write_b32 v2, v1 offset:160\n"
"s_mov_b64 exec, 0x03ff03ff\n"
"v_cvt_pk_bf16_f32 v8, v40, v41\n"
"v_cvt_pk_bf16_f32 v9, v42, v43\n"
"ds_write_b64 v3, v[8:9] offset:0\n"
"v_lshlrev_b32 v10, 1, v3\n"
"ds_write_b128 v10, v[40:43] offset:2560\n"
"s_mov_b64 exec, -1\n"
"s_waitcnt lgkmcnt(1)\n"
".Lr_7_%=:\n"
"s_mul_i32 s76, s48, 10\n"
"s_add_u32 s3, s3, s76\n"
"s_waitcnt lgkmcnt(0)\n"
"s_add_u32 s76, s3, 302\n"
".Lp_8_%=:\n"
"s_memrealtime vcc\n"
"s_waitcnt lgkmcnt(0)\n"
"s_sub_u32 vcc_lo, vcc_lo, s76\n"
"s_cmp_lt_i32 vcc_lo, 0\n"
"s_cbranch_scc1 .Lp_8_%=\n"
"s_add_u32 m0, s75, 65536\n"
"s_mov_b32 s76, 12288\n"
"buffer_load_dwordx4 v44, s[4:7], s76 offen offset:0 nt lds\n"
"buffer_load_dwordx4 v45, s[4:7], s76 offen offset:1024 nt lds\n"
"buffer_load_dwordx4 v46, s[4:7], s76 offen offset:2048 nt lds\n"
"s_add_u32 m0, m0, 3072\n"
"s_nop 0\n"
"buffer_load_dwordx4 v47, s[4:7], s76 offen offset:0 nt lds\n"
"buffer_load_dwordx4 v48, s[4:7], s76 offen offset:1024 nt lds\n"
"buffer_load_dwordx4 v49, s[4:7], s76 offen offset:2048 nt lds\n"
"s_waitcnt lgkmcnt(0)\n"
"s_add_u32 s76, s3, 390\n"
".Lp_9_%=:\n"
"s_memrealtime vcc\n"
"s_waitcnt lgkmcnt(0)\n"
"s_sub_u32 vcc_lo, vcc_lo, s76\n"
"s_cmp_lt_i32 vcc_lo, 0\n"
"s_cbranch_scc1 .Lp_9_%=\n"
"s_add_u32 m0, s75, 71680\n"
"s_mov_b32 s76, 18432\n"
"buffer_load_dwordx4 v44, s[4:7], s76 offen offset:0 nt lds\n"
"buffer_load_dwordx4 v45, s[4:7], s76 offen offset:1024 nt lds\n"
"buffer_load_dwordx4 v46, s[4:7], s76 offen offset:2048 nt lds\n"
"s_add_u32 m0, m0, 3072\n"
"s_nop 0\n"
"buffer_load_dwordx4 v47, s[4:7], s76 offen offset:0 nt lds\n"
"buffer_load_dwordx4 v48, s[4:7], s76 offen offset:1024 nt lds\n"
"buffer_load_dwordx4 v49, s[4:7], s76 offen offset:2048 nt lds\n"
"ds_read_b128 a[0:3], v56 offset:49152\n"
"ds_read_b128 a[4:7], v57 offset:49152\n"
"ds_read_b128 a[8:11], v56 offset:49280\n"
"ds_read_b128 a[12:15], v57 offset:49280\n"
"ds_read_b128 a[16:19], v56 offset:49408\n"
"ds_read_b128 a[20:23], v57 offset:49408\n"
"s_waitcnt vmcnt(13)\n"
"s_bitcmp1_b32 6, s48\n"
"s_cbranch_scc0 .Lr_10_%=\n"
"s_waitcnt vmcnt(12)\n"
".Lr_10_%=:\n"
"s_waitcnt lgkmcnt(0)\n"
"s_add_u32 s76, s3, 479\n"
".Lp_11_%=:\n"
"s_memrealtime vcc\n"
"s_waitcnt lgkmcnt(0)\n"
"s_sub_u32 vcc_lo, vcc_lo, s76\n"
"s_cmp_lt_i32 vcc_lo, 0\n"
"s_cbranch_scc1 .Lp_11_%=\n"
"s_add_u32 m0, s75, 114688\n"
"s_mov_b32 s76, 24576\n"
"buffer_load_dwordx4 v44, s[4:7], s76 offen offset:0 nt lds\n"
"buffer_load_dwordx4 v45, s[4:7], s76 offen offset:1024 nt lds\n"
"buffer_load_dwordx4 v46, s[4:7], s76 offen offset:2048 nt lds\n"
"s_add_u32 m0, m0, 3072\n"
"s_nop 0\n"
"buffer_load_dwordx4 v47, s[4:7], s76 offen offset:0 nt lds\n"
"buffer_load_dwordx4 v48, s[4:7], s76 offen offset:1024 nt lds\n"
"buffer_load_dwordx4 v49, s[4:7], s76 offen offset:2048 nt lds\n"
"ds_read_b128 a[24:27], v56 offset:55296\n"
"ds_read_b128 a[28:31], v57 offset:55296\n"
"ds_read_b128 a[32:35], v56 offset:55424\n"
"ds_read_b128 a[36:39], v57 offset:55424\n"
"ds_read_b128 a[40:43], v56 offset:55552\n"
"ds_read_b128 a[44:47], v57 offset:55552\n"
"s_waitcnt lgkmcnt(0)\n"
"s_barrier\n"
"v_lshrrev_b32 v30, 2, v0\n"
"v_and_b32 v31, 12, v30\n"
"v_add_u32 v30, v30, v31\n"
"v_lshrrev_b32 v31, 1, v30\n"
"v_and_b32 v31, 4, v31\n"
"v_and_or_b32 v31, v30, 2, v31\n"
"v_lshlrev_b32 v31, 1, v31\n"
"v_and_or_b32 v31, v0, 3, v31\n"
"v_lshlrev_b32 v31, 3, v31\n"
"v_mul_u32_u24 v30, 384, v30\n"
"v_add_u32 v28, v30, v31\n"
"v_lshrrev_b32 v30, 2, v0\n"
"v_lshrrev_b32 v31, 1, v30\n"
"v_and_b32 v31, 4, v31\n"
"v_and_or_b32 v31, v30, 2, v31\n"
"v_lshlrev_b32 v31, 1, v31\n"
"v_and_or_b32 v31, v0, 3, v31\n"
"v_lshlrev_b32 v31, 3, v31\n"
"v_mul_u32_u24 v30, 384, v30\n"
"v_add_u32 v29, v30, v31\n"
"s_mul_i32 s70, s48, 3\n"
"s_add_u32 s70, s70, 0\n"
"s_and_b32 s71, s70, 3\n"
"s_lshl_b32 s71, s71, 5\n"
"s_lshr_b32 s70, s70, 2\n"
"s_lshl_b32 s70, s70, 7\n"
"s_add_u32 s70, s70, 10240\n"
"v_xor_b32 v30, s71, v28\n"
"v_add_u32 v30, s70, v30\n"
"ds_read_b64_tr_b16 v[98:99], v30 offset:0\n"
"ds_read_b64_tr_b16 v[100:101], v30 offset:1536\n"
"ds_read_b64_tr_b16 v[102:103], v30 offset:12288\n"
"ds_read_b64_tr_b16 v[104:105], v30 offset:13824\n"
"s_mul_i32 s70, s48, 3\n"
"s_add_u32 s70, s70, 0\n"
"s_and_b32 s71, s70, 3\n"
"s_lshl_b32 s71, s71, 5\n"
"s_lshr_b32 s70, s70, 2\n"
"s_lshl_b32 s70, s70, 7\n"
"s_add_u32 s70, s70, 10240\n"
"v_xor_b32 v31, s71, v29\n"
"v_add_u32 v31, s70, v31\n"
"ds_read_b64_tr_b16 v[106:107], v31 offset:24576\n"
"s_mul_i32 s70, s48, 3\n"
"s_add_u32 s70, s70, 1\n"
"s_and_b32 s71, s70, 3\n"
"s_lshl_b32 s71, s71, 5\n"
"s_lshr_b32 s70, s70, 2\n"
"s_lshl_b32 s70, s70, 7\n"
"s_add_u32 s70, s70, 10240\n"
"v_xor_b32 v30, s71, v28\n"
"v_add_u32 v30, s70, v30\n"
"ds_read_b64_tr_b16 v[108:109], v30 offset:0\n"
"ds_read_b64_tr_b16 v[110:111], v30 offset:1536\n"
"ds_read_b64_tr_b16 v[112:113], v30 offset:12288\n"
"ds_read_b64_tr_b16 v[114:115], v30 offset:13824\n"
"s_mul_i32 s70, s48, 3\n"
"s_add_u32 s70, s70, 1\n"
"s_and_b32 s71, s70, 3\n"
"s_lshl_b32 s71, s71, 5\n"
"s_lshr_b32 s70, s70, 2\n"
"s_lshl_b32 s70, s70, 7\n"
"s_add_u32 s70, s70, 10240\n"
"v_xor_b32 v31, s71, v29\n"
"v_add_u32 v31, s70, v31\n"
"ds_read_b64_tr_b16 v[116:117], v31 offset:24576\n"
"s_mul_i32 s70, s48, 3\n"
"s_add_u32 s70, s70, 2\n"
"s_and_b32 s71, s70, 3\n"
"s_lshl_b32 s71, s71, 5\n"
"s_lshr_b32 s70, s70, 2\n"
"s_lshl_b32 s70, s70, 7\n"
"s_add_u32 s70, s70, 10240\n"
"v_xor_b32 v30, s71, v28\n"
"v_add_u32 v30, s70, v30\n"
"ds_read_b64_tr_b16 v[118:119], v30 offset:0\n"
"ds_read_b64_tr_b16 v[120:121], v30 offset:1536\n"
"ds_read_b64_tr_b16 v[122:123], v30 offset:12288\n"
"ds_read_b64_tr_b16 v[124:125], v30 offset:13824\n"
"s_mul_i32 s70, s48, 3\n"
"s_add_u32 s70, s70, 2\n"
"s_and_b32 s71, s70, 3\n"
"s_lshl_b32 s71, s71, 5\n"
"s_lshr_b32 s70, s70, 2\n"
"s_lshl_b32 s70, s70, 7\n"
"s_add_u32 s70, s70, 10240\n"
"v_xor_b32 v31, s71, v29\n"
"v_add_u32 v31, s70, v31\n"
"ds_read_b64_tr_b16 v[126:127], v31 offset:24576\n"
"s_waitcnt lgkmcnt(0)\n"
"v_and_b32 v2, 1, v0\n"
"v_mul_u32_u24 v2, 160, v2\n"
"v_lshrrev_b32 v3, 4, v0\n"
"v_lshl_add_u32 v1, v3, 4, v2\n"
"v_lshl_add_u32 v2, v3, 3, v2\n"
"ds_read_b128 v[18:21], v1 offset:0\n"
"ds_read_b128 v[22:25], v1 offset:64\n"
"ds_read_b64 v[26:27], v2 offset:128\n"
"s_add_u32 s66, s54, 1\n"
"s_lshl2_add_u32 s70, s50, s48\n"
"s_lshl_b32 s70, s70, 7\n"
"v_lshl_add_u32 v16, v0, 3, s70\n"
"v_add_u32 v17, 266112, v16\n"
"s_nop 1\n"
"v_mov_b32_dpp v16, v17 quad_perm:[0,1,2,3] row_mask:0x2 bank_mask:0xf\n"
"s_lshl_b32 s70, s50, 7\n"
"s_add_u32 s70, s70, 3584\n"
"s_mov_b32 exec_lo, 0\n"
"v_lshl_add_u32 v16, v0, 4, s70\n"
"s_mov_b32 exec_lo, -1\n"
"v_mov_b32 v11, s66\n"
"s_waitcnt lgkmcnt(2)\n"
"v_mfma_f32_16x16x32_bf16 v[28:31], v[18:21], v[98:101], 0\n"
"v_mfma_f32_16x16x32_bf16 v[32:35], v[18:21], v[108:111], 0\n"
"v_mfma_f32_16x16x32_bf16 v[36:39], v[18:21], v[118:121], 0\n"
"v_mfma_f32_16x16x32_bf16 v[40:43], v[18:21], v[18:21], 0\n"
"s_waitcnt lgkmcnt(1)\n"
"v_mfma_f32_16x16x32_bf16 v[28:31], v[22:25], v[102:105], v[28:31]\n"
"v_mfma_f32_16x16x32_bf16 v[32:35], v[22:25], v[112:115], v[32:35]\n"
"v_mfma_f32_16x16x32_bf16 v[36:39], v[22:25], v[122:125], v[36:39]\n"
"v_mfma_f32_16x16x32_bf16 v[40:43], v[22:25], v[22:25], v[40:43]\n"
"s_waitcnt lgkmcnt(0)\n"
"v_mfma_f32_16x16x16_bf16 v[28:31], v[26:27], v[106:107], v[28:31]\n"
"v_mfma_f32_16x16x16_bf16 v[32:35], v[26:27], v[116:117], v[32:35]\n"
"v_mfma_f32_16x16x16_bf16 v[36:39], v[26:27], v[126:127], v[36:39]\n"
"v_mfma_f32_16x16x16_bf16 v[40:43], v[26:27], v[26:27], v[40:43]\n"
"v_mov_b32_dpp v28, v29 quad_perm:[0,1,2,3] row_mask:0x2 bank_mask:0xf\n"
"v_mov_b32_dpp v32, v33 quad_perm:[0,1,2,3] row_mask:0x2 bank_mask:0xf\n"
"s_nop 1\n"
"v_mov_b32_dpp v36, v37 quad_perm:[0,1,2,3] row_mask:0x2 bank_mask:0xf\n"
"v_mov_b32_dpp v29, v28 quad_perm:[1,1,3,3] row_mask:0xf bank_mask:0xf\n"
"v_mov_b32_dpp v33, v32 quad_perm:[1,1,3,3] row_mask:0xf bank_mask:0xf\n"
"v_mov_b32_dpp v37, v36 quad_perm:[1,1,3,3] row_mask:0xf bank_mask:0xf\n"
"v_cvt_pk_bf16_f32 v8, v28, v29\n"
"v_cvt_pk_bf16_f32 v9, v32, v33\n"
"v_cvt_pk_bf16_f32 v10, v36, v37\n"
"v_mov_b32_dpp v8, v40 quad_perm:[0,1,2,3] row_mask:0x4 bank_mask:0xf\n"
"s_waitcnt vmcnt(12)\n"
"s_waitcnt lgkmcnt(0)\n"
"s_add_u32 s76, s3, 567\n"
".Lp_12_%=:\n"
"s_memrealtime vcc\n"
"s_waitcnt lgkmcnt(0)\n"
"s_sub_u32 vcc_lo, vcc_lo, s76\n"
"s_cmp_lt_i32 vcc_lo, 0\n"
"s_cbranch_scc1 .Lp_12_%=\n"
"s_add_u32 m0, s75, 120832\n"
"s_mov_b32 s76, 30720\n"
"buffer_load_dwordx4 v44, s[4:7], s76 offen offset:0 nt lds\n"
"buffer_load_dwordx4 v45, s[4:7], s76 offen offset:1024 nt lds\n"
"buffer_load_dwordx4 v46, s[4:7], s76 offen offset:2048 nt lds\n"
"s_add_u32 m0, m0, 3072\n"
"s_nop 0\n"
"buffer_load_dwordx4 v47, s[4:7], s76 offen offset:0 nt lds\n"
"buffer_load_dwordx4 v48, s[4:7], s76 offen offset:1024 nt lds\n"
"buffer_load_dwordx4 v49, s[4:7], s76 offen offset:2048 nt lds\n"
"ds_read_b128 a[48:51], v56 offset:0\n"
"ds_read_b128 a[52:55], v57 offset:0\n"
"ds_read_b128 a[56:59], v56 offset:128\n"
"ds_read_b128 a[60:63], v57 offset:128\n"
"ds_read_b128 a[64:67], v56 offset:256\n"
"ds_read_b128 a[68:71], v57 offset:256\n"
"v_lshl_add_u32 v1, s48, 7, v0\n"
"v_lshlrev_b32 v1, 4, v1\n"
"ds_write_b128 v1, v[120:123] offset:47104\n"
"ds_write_b128 v1, v[124:127] offset:48128\n"
"s_waitcnt vmcnt(12)\n"
"s_waitcnt lgkmcnt(0)\n"
"s_add_u32 s76, s3, 656\n"
".Lp_13_%=:\n"
"s_memrealtime vcc\n"
"s_waitcnt lgkmcnt(0)\n"
"s_sub_u32 vcc_lo, vcc_lo, s76\n"
"s_cmp_lt_i32 vcc_lo, 0\n"
"s_cbranch_scc1 .Lp_13_%=\n"
"s_add_u32 m0, s75, 65536\n"
"s_mov_b32 s76, 36864\n"
"buffer_load_dwordx4 v44, s[4:7], s76 offen offset:0 nt lds\n"
"buffer_load_dwordx4 v45, s[4:7], s76 offen offset:1024 nt lds\n"
"buffer_load_dwordx4 v46, s[4:7], s76 offen offset:2048 nt lds\n"
"s_add_u32 m0, m0, 3072\n"
"s_nop 0\n"
"buffer_load_dwordx4 v47, s[4:7], s76 offen offset:0 nt lds\n"
"buffer_load_dwordx4 v48, s[4:7], s76 offen offset:1024 nt lds\n"
"buffer_load_dwordx4 v49, s[4:7], s76 offen offset:2048 nt lds\n"
"ds_read_b128 a[72:75], v56 offset:6144\n"
"ds_read_b128 a[76:79], v57 offset:6144\n"
"ds_read_b128 a[80:83], v56 offset:6272\n"
"ds_read_b128 a[84:87], v57 offset:6272\n"
"ds_read_b128 a[88:91], v56 offset:6400\n"
"ds_read_b128 a[92:95], v57 offset:6400\n"
"s_cmp_eq_u32 s48, 0\n"
"s_cbranch_scc0 .Lr_14_%=\n"
"v_lshlrev_b32 v7, 4, v0\n"
"s_mov_b32 s73, 5373952\n"
"s_add_u32 s66, s54, 11\n"
"buffer_load_dwordx4 v[24:27], v7, s[40:43], s73 offen offset:0 sc1\n"
"buffer_load_dwordx4 v[28:31], v7, s[40:43], s73 offen offset:1024 sc1\n"
".Lr_14_%=:\n"
"s_waitcnt vmcnt(14)\n"
"s_cmp_lg_u32 s48, 0\n"
"s_cbranch_scc0 .Lr_15_%=\n"
"s_waitcnt vmcnt(12)\n"
".Lr_15_%=:\n"
"s_waitcnt lgkmcnt(0)\n"
"s_add_u32 s76, s3, 744\n"
".Lp_16_%=:\n"
"s_memrealtime vcc\n"
"s_waitcnt lgkmcnt(0)\n"
"s_sub_u32 vcc_lo, vcc_lo, s76\n"
"s_cmp_lt_i32 vcc_lo, 0\n"
"s_cbranch_scc1 .Lp_16_%=\n"
"s_add_u32 m0, s75, 71680\n"
"s_mov_b32 s76, 43008\n"
"buffer_load_dwordx4 v44, s[4:7], s76 offen offset:0 nt lds\n"
"buffer_load_dwordx4 v45, s[4:7], s76 offen offset:1024 nt lds\n"
"buffer_load_dwordx4 v46, s[4:7], s76 offen offset:2048 nt lds\n"
"s_add_u32 m0, m0, 3072\n"
"s_nop 0\n"
"buffer_load_dwordx4 v47, s[4:7], s76 offen offset:0 nt lds\n"
"buffer_load_dwordx4 v48, s[4:7], s76 offen offset:1024 nt lds\n"
"buffer_load_dwordx4 v49, s[4:7], s76 offen offset:2048 nt lds\n"
"ds_read_b128 a[96:99], v56 offset:49152\n"
"ds_read_b128 a[100:103], v57 offset:49152\n"
"ds_read_b128 a[104:107], v56 offset:49280\n"
"ds_read_b128 a[108:111], v57 offset:49280\n"
"ds_read_b128 a[112:115], v56 offset:49408\n"
"ds_read_b128 a[116:119], v57 offset:49408\n"
"s_waitcnt vmcnt(14)\n"
"s_cmp_lg_u32 s48, 0\n"
"s_cbranch_scc0 .Lr_17_%=\n"
"s_waitcnt vmcnt(12)\n"
".Lr_17_%=:\n"
"s_waitcnt lgkmcnt(0)\n"
"s_add_u32 s76, s3, 833\n"
".Lp_18_%=:\n"
"s_memrealtime vcc\n"
"s_waitcnt lgkmcnt(0)\n"
"s_sub_u32 vcc_lo, vcc_lo, s76\n"
"s_cmp_lt_i32 vcc_lo, 0\n"
"s_cbranch_scc1 .Lp_18_%=\n"
"s_add_u32 m0, s75, 114688\n"
"s_mov_b32 s76, 0\n"
"buffer_load_dwordx4 v50, s[8:11], s76 offen offset:0 nt lds\n"
"buffer_load_dwordx4 v51, s[8:11], s76 offen offset:1024 nt lds\n"
"buffer_load_dwordx4 v52, s[8:11], s76 offen offset:2048 nt lds\n"
"s_add_u32 m0, m0, 3072\n"
"s_nop 0\n"
"buffer_load_dwordx4 v53, s[8:11], s76 offen offset:0 nt lds\n"
"buffer_load_dwordx4 v54, s[8:11], s76 offen offset:1024 nt lds\n"
"buffer_load_dwordx4 v55, s[8:11], s76 offen offset:2048 nt lds\n"
"ds_read_b128 a[120:123], v56 offset:55296\n"
"ds_read_b128 a[124:127], v57 offset:55296\n"
"ds_read_b128 a[128:131], v56 offset:55424\n"
"ds_read_b128 a[132:135], v57 offset:55424\n"
"ds_read_b128 a[136:139], v56 offset:55552\n"
"ds_read_b128 a[140:143], v57 offset:55552\n"
"s_cmp_eq_u32 s48, 0\n"
"s_cbranch_scc0 .Lr_19_%=\n"
".Lgb_20_%=:\n"
"s_waitcnt vmcnt(12)\n"
"v_bitop3_b32 v1, v25, v27, s66 bitop3:0x7e\n"
"v_bitop3_b32 v1, v29, v1, s66 bitop3:0xde\n"
"v_bitop3_b32 v1, v31, v1, s66 bitop3:0xde\n"
"v_cmp_ne_u32 vcc, 0, v1\n"
"s_cbranch_vccnz .Lgr_21_%=\n"
".Lgo_22_%=:\n"
"s_mov_b32 s77, 0\n"
"v_and_b32 v1, 1, v24\n"
"v_cmp_ne_u32 vcc, 0, v1\n"
"s_nop 0\n"
"s_bcnt1_i32_b64 s78, vcc\n"
"s_add_u32 s77, s77, s78\n"
"v_and_b32 v1, 1, v26\n"
"v_cmp_ne_u32 vcc, 0, v1\n"
"s_nop 0\n"
"s_bcnt1_i32_b64 s78, vcc\n"
"s_add_u32 s77, s77, s78\n"
"v_and_b32 v1, 1, v28\n"
"v_cmp_ne_u32 vcc, 0, v1\n"
"s_nop 0\n"
"s_bcnt1_i32_b64 s78, vcc\n"
"s_add_u32 s77, s77, s78\n"
"v_and_b32 v1, 1, v30\n"
"v_cmp_ne_u32 vcc, 0, v1\n"
"s_nop 0\n"
"s_bcnt1_i32_b64 s78, vcc\n"
"s_add_u32 s77, s77, s78\n"
"s_lshl_b32 s78, s3, 1\n"
"v_sub_u32 v24, s78, v24\n"
"v_sub_u32 v26, s78, v26\n"
"v_sub_u32 v28, s78, v28\n"
"v_sub_u32 v30, s78, v30\n"
"s_movk_i32 s78, -600\n"
"v_mov_b32 v1, 600\n"
"v_med3_i32 v24, v24, s78, v1\n"
"v_med3_i32 v26, v26, s78, v1\n"
"v_med3_i32 v28, v28, s78, v1\n"
"v_med3_i32 v30, v30, s78, v1\n"
"v_add3_u32 v2, v24, v26, v28\n"
"v_add_u32 v2, v2, v30\n"
"s_nop 1\n"
"v_add_u32_dpp v2, v2, v2 row_ror:1 row_mask:0xf bank_mask:0xf\n"
"s_nop 1\n"
"v_add_u32_dpp v2, v2, v2 row_ror:2 row_mask:0xf bank_mask:0xf\n"
"s_nop 1\n"
"v_add_u32_dpp v2, v2, v2 row_ror:4 row_mask:0xf bank_mask:0xf\n"
"s_nop 1\n"
"v_add_u32_dpp v2, v2, v2 row_ror:8 row_mask:0xf bank_mask:0xf\n"
"v_mov_b32 v1, v2\n"
"s_nop 1\n"
"v_permlane16_swap_b32 v2, v1\n"
"v_add_u32 v2, v2, v1\n"
"v_mov_b32 v1, v2\n"
"s_nop 1\n"
"v_permlane32_swap_b32 v2, v1\n"
"v_add_u32 v2, v2, v1\n"
"v_ashrrev_i32 v2, 9, v2\n"
"s_cmp_ge_u32 s77, 128\n"
"s_cselect_b32 s77, 1, 0\n"
"v_mov_b32 v1, s77\n"
"s_mov_b64 exec, 1\n"
"ds_write2_b32 v0, v1, v2 offset0:82 offset1:83\n"
"s_mov_b64 exec, -1\n"
"s_add_u32 s66, s54, 1\n"
".Lr_19_%=:\n"
"s_waitcnt lgkmcnt(0)\n"
"s_barrier\n"
"v_mov_b32 v1, 0\n"
"ds_read_b64 v[2:3], v1 offset:328\n"
"s_waitcnt lgkmcnt(0)\n"
"v_readfirstlane_b32 s79, v2\n"
"v_readfirstlane_b32 s78, v3\n"
"s_nop 0\n"
"s_sub_u32 s3, s3, s78\n"
"s_xor_b32 s79, s79, s51\n"
"s_lshl_b32 s59, s79, 13\n"
"s_mov_b32 s0, s79\n"
"s_lshl_b32 s1, s0, 13\n"
"s_and_b32 s77, s49, 15\n"
"s_lshl_b32 s56, s77, 14\n"
"s_add_u32 s56, s56, s59\n"
"s_add_u32 s56, s56, 4194304\n"
"s_add_u32 s57, s56, 262144\n"
"s_lshl_b32 s58, s50, 14\n"
"s_add_u32 s58, s58, 4718592\n"
"s_lshl3_add_u32 s62, s77, s50\n"
"s_lshl_b32 s62, s62, 15\n"
"s_lshr_b32 s77, s77, 3\n"
"s_xor_b32 s77, s77, s79\n"
"s_lshl_b32 s77, s77, 13\n"
"s_add_u32 s62, s62, s77\n"
"s_lshr_b32 s77, s48, 1\n"
"s_lshl_b32 s78, s77, 14\n"
"s_add_u32 s62, s62, s78\n"
"s_lshl1_add_u32 s60, s52, s77\n"
"s_lshl_b32 s60, s60, 14\n"
"s_add_u32 s60, s60, s1\n"
"s_and_b32 s77, s48, 1\n"
"s_lshl_b32 s78, s77, 12\n"
"s_add_u32 s62, s62, s78\n"
"s_mul_i32 s77, s77, 384\n"
"s_add_u32 s60, s60, s77\n"
"s_add_u32 s60, s60, 5242880\n"
"s_bfe_u32 s61, s49, 0x10003\n"
"s_lshr_b32 s77, s50, 2\n"
"s_lshl1_add_u32 s61, s61, s77\n"
"s_lshl_b32 s61, s61, 14\n"
"s_and_b32 s77, s49, 7\n"
"s_mul_i32 s77, s77, 768\n"
"s_add_u32 s61, s61, s77\n"
"s_and_b32 s77, s50, 3\n"
"s_mul_i32 s77, s77, 192\n"
"s_add_u32 s61, s61, s77\n"
"s_add_u32 s61, s61, 5242880\n"
"s_add_u32 s61, s61, s59\n"
"s_lshl_b32 s77, s48, 6\n"
"s_add_u32 s61, s61, s77\n"
"s_sub_u32 s61, s61, 64\n"
"s_lshl_b32 s63, s52, 21\n"
"s_lshr_b32 s77, s53, 5\n"
"s_lshl_b32 s77, s77, 14\n"
"s_add_u32 s63, s63, s77\n"
"s_xor_b32 s77, s0, s52\n"
"s_lshl_b32 s77, s77, 13\n"
"s_add_u32 s63, s63, s77\n"
"s_and_b32 s77, s53, 31\n"
"s_lshl_b32 s77, s77, 8\n"
"s_add_u32 s63, s63, s77\n"
"s_lshl_b32 s77, s48, 16\n"
"s_add_u32 s63, s63, s77\n"
"s_lshl_b32 s64, s52, 17\n"
"s_lshl4_add_u32 s64, s53, s64\n"
"s_add_u32 s64, s64, s1\n"
"s_add_u32 s64, s64, 4199424\n"
"s_mov_b32 s78, 0x55555555\n"
"s_cmp_eq_u32 s48, 3\n"
"s_cselect_b32 s79, 3, 0\n"
"s_mov_b64 exec, s[78:79]\n"
"buffer_store_dwordx4 v[8:11], v16, s[40:43], s56 offen offset:0 sc1\n"
"s_mov_b64 exec, -1\n"
"s_waitcnt vmcnt(13)\n"
"s_waitcnt lgkmcnt(0)\n"
"s_add_u32 s76, s3, 921\n"
".Lp_23_%=:\n"
"s_memrealtime vcc\n"
"s_waitcnt lgkmcnt(0)\n"
"s_sub_u32 vcc_lo, vcc_lo, s76\n"
"s_cmp_lt_i32 vcc_lo, 0\n"
"s_cbranch_scc1 .Lp_23_%=\n"
"s_add_u32 m0, s75, 120832\n"
"s_mov_b32 s76, 6144\n"
"buffer_load_dwordx4 v50, s[8:11], s76 offen offset:0 nt lds\n"
"buffer_load_dwordx4 v51, s[8:11], s76 offen offset:1024 nt lds\n"
"buffer_load_dwordx4 v52, s[8:11], s76 offen offset:2048 nt lds\n"
"s_add_u32 m0, m0, 3072\n"
"s_nop 0\n"
"buffer_load_dwordx4 v53, s[8:11], s76 offen offset:0 nt lds\n"
"buffer_load_dwordx4 v54, s[8:11], s76 offen offset:1024 nt lds\n"
"buffer_load_dwordx4 v55, s[8:11], s76 offen offset:2048 nt lds\n"
"ds_read_b128 a[144:147], v56 offset:0\n"
"ds_read_b128 a[148:151], v57 offset:0\n"
"ds_read_b128 a[152:155], v56 offset:128\n"
"ds_read_b128 a[156:159], v57 offset:128\n"
"ds_read_b128 a[160:163], v56 offset:256\n"
"ds_read_b128 a[164:167], v57 offset:256\n"
"s_cmp_eq_u32 s48, 2\n"
"s_cbranch_scc0 .Lr_24_%=\n"
"v_lshlrev_b32 v1, 7, v0\n"
"s_mov_b32 m0, 768\n"
"s_add_u32 s70, s56, 5120\n"
"s_mov_b64 exec, 0xffffff\n"
"buffer_load_dword v1, s[40:43], s70 offen sc1 lds\n"
"s_mov_b64 exec, -1\n"
".Lr_24_%=:\n"
"s_waitcnt vmcnt(14)\n"
"s_cmp_lg_u32 s48, 2\n"
"s_cbranch_scc0 .Lr_25_%=\n"
"s_waitcnt vmcnt(13)\n"
".Lr_25_%=:\n"
"s_waitcnt lgkmcnt(0)\n"
"s_add_u32 s76, s3, 1010\n"
".Lp_26_%=:\n"
"s_memrealtime vcc\n"
"s_waitcnt lgkmcnt(0)\n"
"s_sub_u32 vcc_lo, vcc_lo, s76\n"
"s_cmp_lt_i32 vcc_lo, 0\n"
"s_cbranch_scc1 .Lp_26_%=\n"
"s_add_u32 m0, s75, 65536\n"
"s_mov_b32 s76, 12288\n"
"buffer_load_dwordx4 v50, s[8:11], s76 offen offset:0 nt lds\n"
"buffer_load_dwordx4 v51, s[8:11], s76 offen offset:1024 nt lds\n"
"buffer_load_dwordx4 v52, s[8:11], s76 offen offset:2048 nt lds\n"
"s_add_u32 m0, m0, 3072\n"
"s_nop 0\n"
"buffer_load_dwordx4 v53, s[8:11], s76 offen offset:0 nt lds\n"
"buffer_load_dwordx4 v54, s[8:11], s76 offen offset:1024 nt lds\n"
"buffer_load_dwordx4 v55, s[8:11], s76 offen offset:2048 nt lds\n"
"ds_read_b128 a[168:171], v56 offset:6144\n"
"ds_read_b128 a[172:175], v57 offset:6144\n"
"ds_read_b128 a[176:179], v56 offset:6272\n"
"ds_read_b128 a[180:183], v57 offset:6272\n"
"ds_read_b128 a[184:187], v56 offset:6400\n"
"ds_read_b128 a[188:191], v57 offset:6400\n"
"s_cmp_eq_u32 s48, 0\n"
"s_cbranch_scc0 .Lr_27_%=\n"
"v_and_b32 v38, 15, v0\n"
"v_lshlrev_b32 v38, 1, v38\n"
"s_mov_b32 s70, 0x44ee44\n"
"v_bfe_u32 v1, s70, v38, 2\n"
"s_mov_b32 s70, 0xf5a0a0\n"
"v_bfe_u32 v38, s70, v38, 2\n"
"v_mul_u32_u24 v1, 5, v1\n"
"v_lshlrev_b32 v2, 2, v38\n"
"v_lshlrev_b32 v1, v2, v1\n"
"v_lshlrev_b32 v38, v38, 1\n"
"v_bfe_i32 v2, v38, 0, 1\n"
"v_and_b32 v2, 0x3f80, v2\n"
"v_bfe_i32 v3, v38, 1, 1\n"
"v_and_b32 v3, 0x3f80, v3\n"
"v_bfe_i32 v4, v38, 2, 1\n"
"v_and_b32 v4, 0x3f80, v4\n"
"v_bfe_i32 v5, v38, 3, 1\n"
"v_and_b32 v5, 0x3f80, v5\n"
"v_and_b32 v7, 15, v0\n"
"v_bfe_u32 v38, v0, 4, 1\n"
"v_lshl_add_u32 v7, v7, 1, v38\n"
"v_and_b32 v38, 32, v0\n"
"v_add_lshl_u32 v7, v7, v38, 4\n"
"v_mov_b64 v[28:29], 0\n"
"v_mov_b64 v[30:31], 0\n"
"v_bfe_u32 v38, v0, 2, 2\n"
"v_lshrrev_b32 v36, 4, v0\n"
"v_mad_u32_u24 v38, v38, 3, v36\n"
"v_and_b32 v36, 3, v0\n"
"v_lshl_add_u32 v36, v38, 3, v36\n"
"v_lshlrev_b32 v36, 2, v36\n"
"v_lshrrev_b32 v42, 4, v0\n"
"v_and_b32 v38, 1, v0\n"
"v_lshl_add_u32 v42, v42, 1, v38\n"
"v_lshlrev_b32 v42, 4, v42\n"
"v_and_b32 v37, 14, v0\n"
"v_and_b32 v39, 1, v0\n"
"v_lshl_add_u32 v37, v37, 2, v39\n"
"v_lshlrev_b32 v37, 4, v37\n"
"v_add_u32 v37, 4096, v37\n"
"v_lshlrev_b32 v39, 4, v0\n"
"buffer_load_dwordx4 v[8:11], v7, s[40:43], s56 offen offset:0 sc1\n"
"buffer_load_dwordx4 v[12:15], v7, s[40:43], s56 offen offset:1024 sc1\n"
"buffer_load_dwordx4 v[16:19], v7, s[40:43], s56 offen offset:2048 sc1\n"
"buffer_load_dwordx4 v[20:23], v7, s[40:43], s56 offen offset:3072 sc1\n"
"buffer_load_dwordx4 v[24:27], v37, s[40:43], s56 offen offset:0 sc1\n"
".Lr_27_%=:\n"
"s_cmp_eq_u32 s48, 1\n"
"s_cbranch_scc0 .Lr_28_%=\n"
"v_and_b32 v38, 15, v0\n"
"v_lshlrev_b32 v38, 1, v38\n"
"s_mov_b32 s70, 0x44ee44\n"
"v_bfe_u32 v1, s70, v38, 2\n"
"s_mov_b32 s70, 0xf5a0a0\n"
"v_bfe_u32 v38, s70, v38, 2\n"
"v_mul_u32_u24 v1, 5, v1\n"
"v_lshlrev_b32 v2, 2, v38\n"
"v_lshlrev_b32 v1, v2, v1\n"
"v_lshlrev_b32 v38, v38, 1\n"
"v_bfe_i32 v2, v38, 0, 1\n"
"v_and_b32 v2, 0x3f80, v2\n"
"v_bfe_i32 v3, v38, 1, 1\n"
"v_and_b32 v3, 0x3f80, v3\n"
"v_bfe_i32 v4, v38, 2, 1\n"
"v_and_b32 v4, 0x3f80, v4\n"
"v_bfe_i32 v5, v38, 3, 1\n"
"v_and_b32 v5, 0x3f80, v5\n"
"v_and_b32 v7, 15, v0\n"
"v_bfe_u32 v38, v0, 4, 1\n"
"v_lshl_add_u32 v7, v7, 1, v38\n"
"v_and_b32 v38, 32, v0\n"
"v_add_lshl_u32 v7, v7, v38, 4\n"
"v_mov_b64 v[28:29], 0\n"
"v_mov_b64 v[30:31], 0\n"
"v_add_u32 v7, 4096, v7\n"
"v_lshlrev_b32 v39, 4, v0\n"
"buffer_load_dwordx4 v[8:11], v7, s[40:43], s57 offen offset:0 sc1\n"
"buffer_load_dwordx4 v[12:15], v7, s[40:43], s57 offen offset:1024 sc1\n"
"buffer_load_dwordx4 v[16:19], v7, s[40:43], s57 offen offset:2048 sc1\n"
"buffer_load_dwordx4 v[20:23], v7, s[40:43], s57 offen offset:3072 sc1\n"
".Lr_28_%=:\n"
"s_waitcnt vmcnt(18)\n"
"s_cmp_lg_u32 s48, 0\n"
"s_cbranch_scc0 .Lr_29_%=\n"
"s_waitcnt vmcnt(17)\n"
".Lr_29_%=:\n"
"s_bitcmp1_b32 12, s48\n"
"s_cbranch_scc0 .Lr_30_%=\n"
"s_waitcnt vmcnt(14)\n"
".Lr_30_%=:\n"
"s_cmp_eq_u32 s48, 3\n"
"s_cbranch_scc0 .Lr_31_%=\n"
"s_waitcnt vmcnt(13)\n"
".Lr_31_%=:\n"
"s_waitcnt lgkmcnt(0)\n"
"s_add_u32 s76, s3, 1098\n"
".Lp_32_%=:\n"
"s_memrealtime vcc\n"
"s_waitcnt lgkmcnt(0)\n"
"s_sub_u32 vcc_lo, vcc_lo, s76\n"
"s_cmp_lt_i32 vcc_lo, 0\n"
"s_cbranch_scc1 .Lp_32_%=\n"
"s_add_u32 m0, s75, 71680\n"
"s_mov_b32 s76, 18432\n"
"buffer_load_dwordx4 v50, s[8:11], s76 offen offset:0 nt lds\n"
"buffer_load_dwordx4 v51, s[8:11], s76 offen offset:1024 nt lds\n"
"buffer_load_dwordx4 v52, s[8:11], s76 offen offset:2048 nt lds\n"
"s_add_u32 m0, m0, 3072\n"
"s_nop 0\n"
"buffer_load_dwordx4 v53, s[8:11], s76 offen offset:0 nt lds\n"
"buffer_load_dwordx4 v54, s[8:11], s76 offen offset:1024 nt lds\n"
"buffer_load_dwordx4 v55, s[8:11], s76 offen offset:2048 nt lds\n"
"s_waitcnt vmcnt(17)\n"
"s_cmp_lg_u32 s48, 0\n"
"s_cbranch_scc0 .Lr_33_%=\n"
"s_waitcnt vmcnt(16)\n"
".Lr_33_%=:\n"
"s_bitcmp1_b32 12, s48\n"
"s_cbranch_scc0 .Lr_34_%=\n"
"s_waitcnt vmcnt(13)\n"
".Lr_34_%=:\n"
"s_cmp_eq_u32 s48, 3\n"
"s_cbranch_scc0 .Lr_35_%=\n"
"s_waitcnt vmcnt(12)\n"
".Lr_35_%=:\n"
"ds_read_b64_tr_b16 a[192:193], v58 offset:49152\n"
"ds_read_b64_tr_b16 a[194:195], v59 offset:49152\n"
"ds_read_b64_tr_b16 a[200:201], v60 offset:49152\n"
"ds_read_b64_tr_b16 a[202:203], v61 offset:49152\n"
"ds_read_b64_tr_b16 a[208:209], v62 offset:49152\n"
"ds_read_b64_tr_b16 a[210:211], v63 offset:49152\n"
"ds_read_b64_tr_b16 a[216:217], v64 offset:49152\n"
"ds_read_b64_tr_b16 a[218:219], v65 offset:49152\n"
"ds_read_b64_tr_b16 a[224:225], v58 offset:49280\n"
"ds_read_b64_tr_b16 a[226:227], v59 offset:49280\n"
"ds_read_b64_tr_b16 a[232:233], v60 offset:49280\n"
"ds_read_b64_tr_b16 a[234:235], v61 offset:49280\n"
"ds_read_b64_tr_b16 a[240:241], v62 offset:49280\n"
"ds_read_b64_tr_b16 a[242:243], v63 offset:49280\n"
"ds_read_b64_tr_b16 a[248:249], v64 offset:49280\n"
"ds_read_b64_tr_b16 a[250:251], v65 offset:49280\n"
"ds_read_b64_tr_b16 v[128:129], v58 offset:49408\n"
"ds_read_b64_tr_b16 v[130:131], v59 offset:49408\n"
"ds_read_b64_tr_b16 v[136:137], v60 offset:49408\n"
"ds_read_b64_tr_b16 v[138:139], v61 offset:49408\n"
"ds_read_b64_tr_b16 v[144:145], v62 offset:49408\n"
"ds_read_b64_tr_b16 v[146:147], v63 offset:49408\n"
"ds_read_b64_tr_b16 v[152:153], v64 offset:49408\n"
"ds_read_b64_tr_b16 v[154:155], v65 offset:49408\n"
"s_waitcnt lgkmcnt(0)\n"
"s_add_u32 s76, s3, 1187\n"
".Lp_36_%=:\n"
"s_memrealtime vcc\n"
"s_waitcnt lgkmcnt(0)\n"
"s_sub_u32 vcc_lo, vcc_lo, s76\n"
"s_cmp_lt_i32 vcc_lo, 0\n"
"s_cbranch_scc1 .Lp_36_%=\n"
"s_add_u32 m0, s75, 114688\n"
"s_mov_b32 s76, 24576\n"
"buffer_load_dwordx4 v50, s[8:11], s76 offen offset:0 nt lds\n"
"buffer_load_dwordx4 v51, s[8:11], s76 offen offset:1024 nt lds\n"
"buffer_load_dwordx4 v52, s[8:11], s76 offen offset:2048 nt lds\n"
"s_add_u32 m0, m0, 3072\n"
"s_nop 0\n"
"buffer_load_dwordx4 v53, s[8:11], s76 offen offset:0 nt lds\n"
"buffer_load_dwordx4 v54, s[8:11], s76 offen offset:1024 nt lds\n"
"buffer_load_dwordx4 v55, s[8:11], s76 offen offset:2048 nt lds\n"
"s_cmp_eq_u32 s48, 1\n"
"s_cbranch_scc0 .Lr_37_%=\n"
".Lgb_38_%=:\n"
"s_waitcnt vmcnt(14)\n"
"v_smfmac_f32_16x16x64_bf16 v[28:31], v[2:5], v[8:15], v1\n"
"s_waitcnt vmcnt(12)\n"
"v_smfmac_f32_16x16x64_bf16 v[28:31], v[2:5], v[16:23], v1\n"
"v_bitop3_b32 v38, v11, v15, s66 bitop3:0x7e\n"
"v_bitop3_b32 v38, v19, v38, s66 bitop3:0xde\n"
"v_bitop3_b32 v38, v23, v38, s66 bitop3:0xde\n"
"v_cmp_ne_u32 vcc, 0, v38\n"
"s_cbranch_vccnz .Lgr_39_%=\n"
".Lgo_40_%=:\n"
"s_nop 2\n"
"v_mul_f32 v28, 0xbacccccd, v28\n"
"v_mul_f32 v29, 0xbacccccd, v29\n"
"v_mul_f32 v30, 0xbacccccd, v30\n"
"v_mul_f32 v31, 0xbacccccd, v31\n"
"s_mov_b32 exec_hi, 0xffff\n"
"ds_write_b128 v39, v[28:31] offset:1024\n"
"s_mov_b32 exec_hi, -1\n"
"s_waitcnt lgkmcnt(0)\n"
".Lr_37_%=:\n"
"s_cmp_lg_u32 s48, 0\n"
"s_cbranch_scc0 .Lr_41_%=\n"
"v_lshrrev_b32 v42, 4, v0\n"
"v_and_b32 v38, 1, v0\n"
"v_lshl_add_u32 v42, v42, 1, v38\n"
"v_lshlrev_b32 v42, 4, v42\n"
".Lr_41_%=:\n"
"s_cmp_eq_u32 s48, 0\n"
"s_cbranch_scc0 .Lr_42_%=\n"
".Lgb_43_%=:\n"
"s_waitcnt vmcnt(15)\n"
"v_smfmac_f32_16x16x64_bf16 v[28:31], v[2:5], v[8:15], v1\n"
"s_waitcnt vmcnt(13)\n"
"v_smfmac_f32_16x16x64_bf16 v[28:31], v[2:5], v[16:23], v1\n"
"s_waitcnt vmcnt(12)\n"
"v_add_f32_dpp v24, v24, v24 row_ror:2 row_mask:0xf bank_mask:0xf\n"
"v_bitop3_b32 v38, v11, v15, s66 bitop3:0x7e\n"
"v_bitop3_b32 v38, v19, v38, s66 bitop3:0xde\n"
"v_add_f32_dpp v24, v24, v24 row_ror:4 row_mask:0xf bank_mask:0xf\n"
"v_bitop3_b32 v38, v23, v38, s66 bitop3:0xde\n"
"v_bitop3_b32 v38, v27, v38, s66 bitop3:0xde\n"
"v_add_f32_dpp v24, v24, v24 row_ror:8 row_mask:0xf bank_mask:0xf\n"
"v_cmp_ne_u32 vcc, 0, v38\n"
"s_cbranch_vccnz .Lgr_44_%=\n"
".Lgo_45_%=:\n"
".Lr_42_%=:\n"
"s_barrier\n"
"s_cmp_eq_u32 s48, 0\n"
"s_cbranch_scc0 .Lr_46_%=\n"
"ds_read_b128 v[32:35], v39 offset:1024\n"
"s_waitcnt lgkmcnt(0)\n"
"v_fmac_f32_dpp v28, v24, v32 quad_perm:[1,1,3,3] row_mask:0xf bank_mask:0xf\n"
"v_fmac_f32_dpp v29, v24, v33 quad_perm:[1,1,3,3] row_mask:0xf bank_mask:0xf\n"
"v_fmac_f32_dpp v30, v24, v34 quad_perm:[1,1,3,3] row_mask:0xf bank_mask:0xf\n"
"v_fmac_f32_dpp v31, v24, v35 quad_perm:[1,1,3,3] row_mask:0xf bank_mask:0xf\n"
"v_cvt_pk_bf16_f32 v40, v28, v29\n"
"v_cvt_pk_bf16_f32 v41, v30, v31\n"
"s_mov_b32 exec_hi, 0xffff\n"
"ds_write2_b32 v36, v40, v41 offset0:96 offset1:100\n"
"s_mov_b64 exec, 3\n"
"ds_write_b32 v39, v24 offset:320\n"
"s_mov_b64 exec, -1\n"
".Lr_46_%=:\n"
"s_waitcnt lgkmcnt(0)\n"
"s_barrier\n"
"ds_read_b128 v[8:11], v42 offset:384\n"
"ds_read_b128 v[12:15], v42 offset:512\n"
"ds_read_b128 v[16:19], v42 offset:640\n"
"v_mov_b32 v5, 0\n"
"ds_read2_b32 v[20:21], v5 offset0:80 offset1:84\n"
"v_mov_b64 v[24:25], 0\n"
"v_mov_b64 v[28:29], 0\n"
"v_mov_b64 v[32:33], 0\n"
"v_mov_b64 v[36:37], 0\n"
"v_mov_b64 v[40:41], 0\n"
"s_lshl_b32 s70, s48, 2\n"
"v_and_or_b32 v1, v0, 48, s70\n"
"s_waitcnt lgkmcnt(3)\n"
"v_smfmac_f32_16x16x64_bf16 v[24:27], v[8:11], a[0:7], v6\n"
"s_waitcnt lgkmcnt(2)\n"
"v_smfmac_f32_16x16x64_bf16 v[24:27], v[12:15], a[8:15], v6\n"
"s_waitcnt lgkmcnt(1)\n"
"v_smfmac_f32_16x16x64_bf16 v[24:27], v[16:19], a[16:23], v6\n"
"s_waitcnt lgkmcnt(0)\n"
"v_smfmac_f32_16x16x64_bf16 v[28:31], v[8:11], a[24:31], v6\n"
"v_smfmac_f32_16x16x64_bf16 v[28:31], v[12:15], a[32:39], v6\n"
"v_smfmac_f32_16x16x64_bf16 v[28:31], v[16:19], a[40:47], v6\n"
"v_add_f32_dpp v22, v24, v25 quad_perm:[0,1,2,3] row_mask:0x1 bank_mask:0xf\n"
"v_mov_b64 v[24:25], 0\n"
"v_smfmac_f32_16x16x64_bf16 v[32:35], v[8:11], a[48:55], v6\n"
"v_smfmac_f32_16x16x64_bf16 v[32:35], v[12:15], a[56:63], v6\n"
"v_smfmac_f32_16x16x64_bf16 v[32:35], v[16:19], a[64:71], v6\n"
"v_add_f32_dpp v22, v28, v29 quad_perm:[0,1,2,3] row_mask:0x2 bank_mask:0xf\n"
"v_mov_b64 v[28:29], 0\n"
"v_smfmac_f32_16x16x64_bf16 v[36:39], v[8:11], a[72:79], v6\n"
"v_smfmac_f32_16x16x64_bf16 v[36:39], v[12:15], a[80:87], v6\n"
"v_smfmac_f32_16x16x64_bf16 v[36:39], v[16:19], a[88:95], v6\n"
"v_add_f32_dpp v22, v32, v33 quad_perm:[0,1,2,3] row_mask:0x4 bank_mask:0xf\n"
"v_mov_b64 v[32:33], 0\n"
"v_smfmac_f32_16x16x64_bf16 v[40:43], v[8:11], a[96:103], v6\n"
"v_smfmac_f32_16x16x64_bf16 v[40:43], v[12:15], a[104:111], v6\n"
"v_smfmac_f32_16x16x64_bf16 v[40:43], v[16:19], a[112:119], v6\n"
"v_add_f32_dpp v22, v36, v37 quad_perm:[0,1,2,3] row_mask:0x8 bank_mask:0xf\n"
"v_smfmac_f32_16x16x64_bf16 v[24:27], v[8:11], a[120:127], v6\n"
"v_smfmac_f32_16x16x64_bf16 v[24:27], v[12:15], a[128:135], v6\n"
"v_smfmac_f32_16x16x64_bf16 v[24:27], v[16:19], a[136:143], v6\n"
"v_add_f32_dpp v23, v40, v41 quad_perm:[0,1,2,3] row_mask:0x1 bank_mask:0xf\n"
"v_mul_f32 v21, 0x3acccccd, v21\n"
"v_smfmac_f32_16x16x64_bf16 v[28:31], v[8:11], a[144:151], v6\n"
"v_smfmac_f32_16x16x64_bf16 v[28:31], v[12:15], a[152:159], v6\n"
"v_smfmac_f32_16x16x64_bf16 v[28:31], v[16:19], a[160:167], v6\n"
"v_add_f32_dpp v23, v24, v25 quad_perm:[0,1,2,3] row_mask:0x2 bank_mask:0xf\n"
"v_mul_f32 v20, 0x3acccccd, v20\n"
"v_smfmac_f32_16x16x64_bf16 v[32:35], v[8:11], a[168:175], v6\n"
"v_smfmac_f32_16x16x64_bf16 v[32:35], v[12:15], a[176:183], v6\n"
"v_smfmac_f32_16x16x64_bf16 v[32:35], v[16:19], a[184:191], v6\n"
"v_add_f32_dpp v23, v28, v29 quad_perm:[0,1,2,3] row_mask:0x4 bank_mask:0xf\n"
"v_fma_f32 v20, -v21, v21, v20\n"
"v_max_f32 v20, 0, v20\n"
"v_add_f32 v20, 0x3727c5ac, v20\n"
"s_mov_b32 s70, 0x3f803f80\n"
"s_nop 2\n"
"v_add_f32_dpp v23, v32, v33 quad_perm:[0,1,2,3] row_mask:0x8 bank_mask:0xf\n"
"v_max_f32 v74, v22, v23\n"
"v_rsq_f32 v20, v20\n"
"s_nop 0\n"
"v_max_f32_dpp v74, v74, v74 row_ror:1 row_mask:0xf bank_mask:0xf\n"
"v_mul_f32 v20, 0x3dd53b95, v20\n"
"s_nop 0\n"
"v_max_f32_dpp v74, v74, v74 row_ror:2 row_mask:0xf bank_mask:0xf\n"
"v_mov_b64 v[8:9], 0\n"
"v_mov_b64 v[12:13], 0\n"
"v_max_f32_dpp v74, v74, v74 row_ror:4 row_mask:0xf bank_mask:0xf\n"
"v_mov_b64 v[16:17], 0\n"
"v_mov_b64 v[28:29], 0\n"
"v_max_f32_dpp v74, v74, v74 row_ror:8 row_mask:0xf bank_mask:0xf\n"
"s_nop 1\n"
"v_max_f32_dpp v74, v74, v74 row_bcast:15 row_mask:0xa bank_mask:0xf\n"
"s_nop 1\n"
"v_max_f32_dpp v74, v74, v74 row_bcast:31 row_mask:0xc bank_mask:0xf\n"
"v_mul_f32 v74, v74, v20\n"
"s_nop 0\n"
"v_readlane_b32 s1, v74, 63\n"
"ds_write_b32 v1, v74 offset:3904\n"
"s_nop 0\n"
"v_fma_f32 v22, v22, v20, -s1\n"
"v_fma_f32 v23, v23, v20, -s1\n"
"s_nop 0\n"
"v_exp_f32_dpp v66, v22 quad_perm:[0,2,0,2] row_mask:0xf bank_mask:0xf\n"
"v_exp_f32_dpp v67, v22 quad_perm:[1,3,1,3] row_mask:0xf bank_mask:0xf\n"
"v_exp_f32_dpp v70, v23 quad_perm:[0,2,0,2] row_mask:0xf bank_mask:0xf\n"
"v_exp_f32_dpp v71, v23 quad_perm:[1,3,1,3] row_mask:0xf bank_mask:0xf\n"
"v_cvt_pk_bf16_f32 v66, v66, v67\n"
"v_cvt_pk_bf16_f32 v70, v70, v71\n"
"v_dot2_f32_bf16 v75, v66, s70, 0\n"
"v_mov_b32_dpp v67, v66 row_ror:4 row_mask:0xf bank_mask:0xf\n"
"v_mov_b32_dpp v68, v66 row_ror:8 row_mask:0xf bank_mask:0xf\n"
"v_mov_b32_dpp v69, v66 row_ror:12 row_mask:0xf bank_mask:0xf\n"
"v_dot2_f32_bf16 v75, v70, s70, v75\n"
"v_mov_b32_dpp v71, v70 row_ror:4 row_mask:0xf bank_mask:0xf\n"
"v_mov_b32_dpp v72, v70 row_ror:8 row_mask:0xf bank_mask:0xf\n"
"v_mov_b32_dpp v73, v70 row_ror:12 row_mask:0xf bank_mask:0xf\n"
"s_waitcnt lgkmcnt(0)\n"
"s_barrier\n"
"ds_read_b128 v[24:27], v5 offset:3952\n"
"s_add_u32 s66, s54, 2\n"
"s_lshl_b32 s72, s50, 7\n"
"s_add_u32 s72, s72, s56\n"
"s_add_u32 s72, s72, 6144\n"
"s_waitcnt lgkmcnt(0)\n"
"v_max3_f32 v74, v24, v25, v26\n"
"v_max_f32 v74, v74, v27\n"
"s_cmp_eq_u32 s48, 1\n"
"s_cbranch_scc0 .Lr_47_%=\n"
"v_mov_b32 v24, v74\n"
"v_mov_b32 v25, s66\n"
"s_mov_b64 exec, 1\n"
"buffer_store_dwordx2 v[24:25], off, s[40:43], s72 sc1\n"
"s_mov_b64 exec, -1\n"
".Lr_47_%=:\n"
"v_sub_f32 v76, s1, v74\n"
"v_exp_f32 v76, v76\n"
"s_bitcmp1_b32 12, s48\n"
"s_cbranch_scc0 .Lr_48_%=\n"
"s_waitcnt vmcnt(12)\n"
".Lr_48_%=:\n"
"s_waitcnt lgkmcnt(0)\n"
"s_add_u32 s76, s3, 1275\n"
".Lp_49_%=:\n"
"s_memrealtime vcc\n"
"s_waitcnt lgkmcnt(0)\n"
"s_sub_u32 vcc_lo, vcc_lo, s76\n"
"s_cmp_lt_i32 vcc_lo, 0\n"
"s_cbranch_scc1 .Lp_49_%=\n"
"s_add_u32 m0, s75, 120832\n"
"s_mov_b32 s76, 30720\n"
"buffer_load_dwordx4 v50, s[8:11], s76 offen offset:0 nt lds\n"
"buffer_load_dwordx4 v51, s[8:11], s76 offen offset:1024 nt lds\n"
"buffer_load_dwordx4 v52, s[8:11], s76 offen offset:2048 nt lds\n"
"s_add_u32 m0, m0, 3072\n"
"s_nop 0\n"
"buffer_load_dwordx4 v53, s[8:11], s76 offen offset:0 nt lds\n"
"buffer_load_dwordx4 v54, s[8:11], s76 offen offset:1024 nt lds\n"
"buffer_load_dwordx4 v55, s[8:11], s76 offen offset:2048 nt lds\n"
"s_cmp_eq_u32 s48, 3\n"
"s_cbranch_scc0 .Lr_50_%=\n"
"s_lshr_b32 s70, s2, 3\n"
"s_lshl_b32 s70, s70, 11\n"
"v_lshlrev_b32 v1, 7, v0\n"
"s_mov_b32 m0, 3968\n"
"s_mov_b64 exec, 0xffff\n"
"buffer_load_dword v1, s[44:47], s70 offen lds\n"
"s_mov_b64 exec, -1\n"
".Lr_50_%=:\n"
"s_waitcnt vmcnt(13)\n"
"s_bitcmp1_b32 5, s48\n"
"s_cbranch_scc0 .Lr_51_%=\n"
"s_waitcnt vmcnt(12)\n"
".Lr_51_%=:\n"
"ds_read_b64_tr_b16 a[196:197], v58 offset:0\n"
"ds_read_b64_tr_b16 a[198:199], v59 offset:0\n"
"ds_read_b64_tr_b16 a[204:205], v60 offset:0\n"
"ds_read_b64_tr_b16 a[206:207], v61 offset:0\n"
"ds_read_b64_tr_b16 a[212:213], v62 offset:0\n"
"ds_read_b64_tr_b16 a[214:215], v63 offset:0\n"
"ds_read_b64_tr_b16 a[220:221], v64 offset:0\n"
"ds_read_b64_tr_b16 a[222:223], v65 offset:0\n"
"ds_read_b64_tr_b16 a[228:229], v58 offset:128\n"
"ds_read_b64_tr_b16 a[230:231], v59 offset:128\n"
"ds_read_b64_tr_b16 a[236:237], v60 offset:128\n"
"ds_read_b64_tr_b16 a[238:239], v61 offset:128\n"
"ds_read_b64_tr_b16 a[244:245], v62 offset:128\n"
"ds_read_b64_tr_b16 a[246:247], v63 offset:128\n"
"ds_read_b64_tr_b16 a[252:253], v64 offset:128\n"
"ds_read_b64_tr_b16 a[254:255], v65 offset:128\n"
"ds_read_b64_tr_b16 v[132:133], v58 offset:256\n"
"ds_read_b64_tr_b16 v[134:135], v59 offset:256\n"
"ds_read_b64_tr_b16 v[140:141], v60 offset:256\n"
"ds_read_b64_tr_b16 v[142:143], v61 offset:256\n"
"ds_read_b64_tr_b16 v[148:149], v62 offset:256\n"
"ds_read_b64_tr_b16 v[150:151], v63 offset:256\n"
"ds_read_b64_tr_b16 v[156:157], v64 offset:256\n"
"ds_read_b64_tr_b16 v[158:159], v65 offset:256\n"
"s_waitcnt lgkmcnt(0)\n"
"s_add_u32 s76, s3, 1364\n"
".Lp_52_%=:\n"
"s_memrealtime vcc\n"
"s_waitcnt lgkmcnt(0)\n"
"s_sub_u32 vcc_lo, vcc_lo, s76\n"
"s_cmp_lt_i32 vcc_lo, 0\n"
"s_cbranch_scc1 .Lp_52_%=\n"
"s_add_u32 m0, s75, 65536\n"
"s_mov_b32 s76, 36864\n"
"buffer_load_dwordx4 v50, s[8:11], s76 offen offset:0 nt lds\n"
"buffer_load_dwordx4 v51, s[8:11], s76 offen offset:1024 nt lds\n"
"buffer_load_dwordx4 v52, s[8:11], s76 offen offset:2048 nt lds\n"
"s_add_u32 m0, m0, 3072\n"
"s_nop 0\n"
"buffer_load_dwordx4 v53, s[8:11], s76 offen offset:0 nt lds\n"
"buffer_load_dwordx4 v54, s[8:11], s76 offen offset:1024 nt lds\n"
"buffer_load_dwordx4 v55, s[8:11], s76 offen offset:2048 nt lds\n"
"s_waitcnt vmcnt(13)\n"
"s_bitcmp1_b32 5, s48\n"
"s_cbranch_scc0 .Lr_53_%=\n"
"s_waitcnt vmcnt(12)\n"
".Lr_53_%=:\n"
"s_waitcnt lgkmcnt(0)\n"
"s_add_u32 s76, s3, 1452\n"
".Lp_54_%=:\n"
"s_memrealtime vcc\n"
"s_waitcnt lgkmcnt(0)\n"
"s_sub_u32 vcc_lo, vcc_lo, s76\n"
"s_cmp_lt_i32 vcc_lo, 0\n"
"s_cbranch_scc1 .Lp_54_%=\n"
"s_add_u32 m0, s75, 71680\n"
"s_mov_b32 s76, 43008\n"
"buffer_load_dwordx4 v50, s[8:11], s76 offen offset:0 nt lds\n"
"buffer_load_dwordx4 v51, s[8:11], s76 offen offset:1024 nt lds\n"
"buffer_load_dwordx4 v52, s[8:11], s76 offen offset:2048 nt lds\n"
"s_add_u32 m0, m0, 3072\n"
"s_nop 0\n"
"buffer_load_dwordx4 v53, s[8:11], s76 offen offset:0 nt lds\n"
"buffer_load_dwordx4 v54, s[8:11], s76 offen offset:1024 nt lds\n"
"buffer_load_dwordx4 v55, s[8:11], s76 offen offset:2048 nt lds\n"
"s_waitcnt lgkmcnt(0)\n"
"v_mov_b64 v[8:9], 0\n"
"v_mov_b64 v[12:13], 0\n"
"v_mov_b64 v[16:17], 0\n"
"v_mov_b64 v[28:29], 0\n"
"v_mov_b64 v[20:21], 0\n"
"v_mov_b64 v[22:23], 0\n"
"v_mov_b64 v[24:25], 0\n"
"v_smfmac_f32_16x16x64_bf16 v[8:11], v[66:69], a[192:199], v6\n"
"v_smfmac_f32_16x16x64_bf16 v[12:15], v[66:69], a[200:207], v6\n"
"v_smfmac_f32_16x16x64_bf16 v[16:19], v[66:69], a[208:215], v6\n"
"v_add_f32_dpp v20, v8, v9 quad_perm:[0,1,2,3] row_mask:0x1 bank_mask:0xf\n"
"v_mov_b64 v[8:9], 0\n"
"v_smfmac_f32_16x16x64_bf16 v[28:31], v[66:69], a[216:223], v6\n"
"v_add_f32_dpp v20, v12, v13 quad_perm:[0,1,2,3] row_mask:0x2 bank_mask:0xf\n"
"v_mov_b64 v[12:13], 0\n"
"v_smfmac_f32_16x16x64_bf16 v[8:11], v[66:69], a[224:231], v6\n"
"v_add_f32_dpp v20, v16, v17 quad_perm:[0,1,2,3] row_mask:0x4 bank_mask:0xf\n"
"v_mov_b64 v[16:17], 0\n"
"v_smfmac_f32_16x16x64_bf16 v[12:15], v[66:69], a[232:239], v6\n"
"v_add_f32_dpp v20, v28, v29 quad_perm:[0,1,2,3] row_mask:0x8 bank_mask:0xf\n"
"v_mov_b64 v[28:29], 0\n"
"v_smfmac_f32_16x16x64_bf16 v[16:19], v[66:69], a[240:247], v6\n"
"v_add_f32_dpp v22, v8, v9 quad_perm:[0,1,2,3] row_mask:0x1 bank_mask:0xf\n"
"v_mov_b64 v[8:9], 0\n"
"v_smfmac_f32_16x16x64_bf16 v[28:31], v[66:69], a[248:255], v6\n"
"v_add_f32_dpp v22, v12, v13 quad_perm:[0,1,2,3] row_mask:0x2 bank_mask:0xf\n"
"v_mov_b64 v[12:13], 0\n"
"v_smfmac_f32_16x16x64_bf16 v[8:11], v[66:69], v[128:135], v6\n"
"v_add_f32_dpp v22, v16, v17 quad_perm:[0,1,2,3] row_mask:0x4 bank_mask:0xf\n"
"v_mov_b64 v[16:17], 0\n"
"v_smfmac_f32_16x16x64_bf16 v[12:15], v[66:69], v[136:143], v6\n"
"v_add_f32_dpp v22, v28, v29 quad_perm:[0,1,2,3] row_mask:0x8 bank_mask:0xf\n"
"v_mov_b64 v[28:29], 0\n"
"v_smfmac_f32_16x16x64_bf16 v[16:19], v[66:69], v[144:151], v6\n"
"v_add_f32_dpp v24, v8, v9 quad_perm:[0,1,2,3] row_mask:0x1 bank_mask:0xf\n"
"v_smfmac_f32_16x16x64_bf16 v[28:31], v[66:69], v[152:159], v6\n"
"s_nop 0\n"
"v_add_f32_dpp v24, v12, v13 quad_perm:[0,1,2,3] row_mask:0x2 bank_mask:0xf\n"
"s_nop 1\n"
"v_add_f32_dpp v24, v16, v17 quad_perm:[0,1,2,3] row_mask:0x4 bank_mask:0xf\n"
"s_nop 2\n"
"v_add_f32_dpp v24, v28, v29 quad_perm:[0,1,2,3] row_mask:0x8 bank_mask:0xf\n"
"v_mov_b64 v[8:9], v[20:21]\n"
"v_mov_b64 v[12:13], v[20:21]\n"
"v_mov_b64 v[16:17], v[20:21]\n"
"v_mov_b64 v[28:29], v[20:21]\n"
"s_waitcnt vmcnt(13)\n"
"s_cmp_lg_u32 s48, 3\n"
"s_cbranch_scc0 .Lr_55_%=\n"
"s_waitcnt vmcnt(12)\n"
".Lr_55_%=:\n"
"ds_read_b64_tr_b16 v[160:161], v58 offset:49152\n"
"ds_read_b64_tr_b16 v[162:163], v59 offset:49152\n"
"ds_read_b64_tr_b16 v[168:169], v60 offset:49152\n"
"ds_read_b64_tr_b16 v[170:171], v61 offset:49152\n"
"ds_read_b64_tr_b16 v[176:177], v62 offset:49152\n"
"ds_read_b64_tr_b16 v[178:179], v63 offset:49152\n"
"ds_read_b64_tr_b16 v[184:185], v64 offset:49152\n"
"ds_read_b64_tr_b16 v[186:187], v65 offset:49152\n"
"ds_read_b64_tr_b16 v[192:193], v58 offset:49280\n"
"ds_read_b64_tr_b16 v[194:195], v59 offset:49280\n"
"ds_read_b64_tr_b16 v[200:201], v60 offset:49280\n"
"ds_read_b64_tr_b16 v[202:203], v61 offset:49280\n"
"ds_read_b64_tr_b16 v[208:209], v62 offset:49280\n"
"ds_read_b64_tr_b16 v[210:211], v63 offset:49280\n"
"ds_read_b64_tr_b16 v[216:217], v64 offset:49280\n"
"ds_read_b64_tr_b16 v[218:219], v65 offset:49280\n"
"ds_read_b64_tr_b16 v[224:225], v58 offset:49408\n"
"ds_read_b64_tr_b16 v[226:227], v59 offset:49408\n"
"ds_read_b64_tr_b16 v[232:233], v60 offset:49408\n"
"ds_read_b64_tr_b16 v[234:235], v61 offset:49408\n"
"ds_read_b64_tr_b16 v[240:241], v62 offset:49408\n"
"ds_read_b64_tr_b16 v[242:243], v63 offset:49408\n"
"ds_read_b64_tr_b16 v[248:249], v64 offset:49408\n"
"ds_read_b64_tr_b16 v[250:251], v65 offset:49408\n"
"s_waitcnt lgkmcnt(0)\n"
"s_cmp_lg_u32 s48, 0\n"
"s_cbranch_scc0 .Lr_56_%=\n"
"s_add_u32 s76, s3, 1527\n"
".Lp_57_%=:\n"
"s_memrealtime vcc\n"
"s_waitcnt lgkmcnt(0)\n"
"s_sub_u32 vcc_lo, vcc_lo, s76\n"
"s_cmp_lt_i32 vcc_lo, 0\n"
"s_cbranch_scc1 .Lp_57_%=\n"
".Lr_56_%=:\n"
"s_cmp_lg_u32 s48, 0\n"
"s_cbranch_scc0 .Lr_58_%=\n"
"s_sub_u32 s70, s48, 1\n"
"s_mul_i32 s71, s70, 81920\n"
"s_mul_i32 s70, s70, 12288\n"
"s_movk_i32 s72, 1120\n"
"v_mul_u32_u24 v77, 205, v0\n"
"v_lshlrev_b32 v78, 4, v0\n"
"s_add_u32 m0, s70, 126976\n"
"v_add_u32 v79, 0, v77\n"
"v_lshrrev_b32 v79, 11, v79\n"
"v_mad_u32_u24 v79, v79, s72, v78\n"
"buffer_load_dwordx4 v79, s[16:19], s71 offen offset:0 nt lds\n"
"v_add_u32 v79, 13120, v77\n"
"v_lshrrev_b32 v79, 11, v79\n"
"v_mad_u32_u24 v79, v79, s72, v78\n"
"buffer_load_dwordx4 v79, s[16:19], s71 offen offset:1024 nt lds\n"
"v_add_u32 v79, 26240, v77\n"
"v_lshrrev_b32 v79, 11, v79\n"
"v_mad_u32_u24 v79, v79, s72, v78\n"
"buffer_load_dwordx4 v79, s[16:19], s71 offen offset:2048 nt lds\n"
"v_add_u32 v79, 39360, v77\n"
"v_lshrrev_b32 v79, 11, v79\n"
"v_mad_u32_u24 v79, v79, s72, v78\n"
"buffer_load_dwordx4 v79, s[16:19], s71 offen offset:3072 nt lds\n"
"s_add_u32 m0, s70, 131072\n"
"s_add_u32 s71, s71, 4096\n"
"v_add_u32 v79, 52480, v77\n"
"v_lshrrev_b32 v79, 11, v79\n"
"v_mad_u32_u24 v79, v79, s72, v78\n"
"buffer_load_dwordx4 v79, s[16:19], s71 offen offset:0 nt lds\n"
"v_add_u32 v79, 65600, v77\n"
"v_lshrrev_b32 v79, 11, v79\n"
"v_mad_u32_u24 v79, v79, s72, v78\n"
"buffer_load_dwordx4 v79, s[16:19], s71 offen offset:1024 nt lds\n"
".Lr_58_%=:\n"
"s_waitcnt vmcnt(12)\n"
"s_cmp_eq_u32 s48, 0\n"
"s_cbranch_scc0 .Lr_59_%=\n"
"s_waitcnt vmcnt(6)\n"
".Lr_59_%=:\n"
"s_waitcnt lgkmcnt(0)\n"
"s_cmp_lg_u32 s48, 0\n"
"s_cbranch_scc0 .Lr_60_%=\n"
"s_add_u32 s76, s3, 1527\n"
".Lp_61_%=:\n"
"s_memrealtime vcc\n"
"s_waitcnt lgkmcnt(0)\n"
"s_sub_u32 vcc_lo, vcc_lo, s76\n"
"s_cmp_lt_i32 vcc_lo, 0\n"
"s_cbranch_scc1 .Lp_61_%=\n"
".Lr_60_%=:\n"
"s_cmp_lg_u32 s48, 0\n"
"s_cbranch_scc0 .Lr_62_%=\n"
"s_sub_u32 s70, s48, 1\n"
"s_mul_i32 s71, s70, 81920\n"
"s_mul_i32 s70, s70, 12288\n"
"s_movk_i32 s72, 1120\n"
"v_mul_u32_u24 v77, 205, v0\n"
"v_lshlrev_b32 v78, 4, v0\n"
"s_add_u32 s71, s71, 4096\n"
"s_add_u32 m0, s70, 131072\n"
"v_add_u32 v79, 78720, v77\n"
"v_lshrrev_b32 v79, 11, v79\n"
"v_mad_u32_u24 v79, v79, s72, v78\n"
"buffer_load_dwordx4 v79, s[16:19], s71 offen offset:2048 nt lds\n"
"v_add_u32 v79, 91840, v77\n"
"v_lshrrev_b32 v79, 11, v79\n"
"v_mad_u32_u24 v79, v79, s72, v78\n"
"buffer_load_dwordx4 v79, s[16:19], s71 offen offset:3072 nt lds\n"
"s_add_u32 m0, s70, 135168\n"
"s_add_u32 s71, s71, 4096\n"
"v_add_u32 v79, 104960, v77\n"
"v_lshrrev_b32 v79, 11, v79\n"
"v_mad_u32_u24 v79, v79, s72, v78\n"
"buffer_load_dwordx4 v79, s[16:19], s71 offen offset:0 nt lds\n"
"v_add_u32 v79, 118080, v77\n"
"v_lshrrev_b32 v79, 11, v79\n"
"v_mad_u32_u24 v79, v79, s72, v78\n"
"buffer_load_dwordx4 v79, s[16:19], s71 offen offset:1024 nt lds\n"
".Lr_62_%=:\n"
"s_waitcnt vmcnt(10)\n"
"s_cmp_eq_u32 s48, 0\n"
"s_cbranch_scc0 .Lr_63_%=\n"
"s_waitcnt vmcnt(0)\n"
".Lr_63_%=:\n"
"ds_read_b64_tr_b16 v[164:165], v58 offset:0\n"
"ds_read_b64_tr_b16 v[166:167], v59 offset:0\n"
"ds_read_b64_tr_b16 v[172:173], v60 offset:0\n"
"ds_read_b64_tr_b16 v[174:175], v61 offset:0\n"
"ds_read_b64_tr_b16 v[180:181], v62 offset:0\n"
"ds_read_b64_tr_b16 v[182:183], v63 offset:0\n"
"ds_read_b64_tr_b16 v[188:189], v64 offset:0\n"
"ds_read_b64_tr_b16 v[190:191], v65 offset:0\n"
"ds_read_b64_tr_b16 v[196:197], v58 offset:128\n"
"ds_read_b64_tr_b16 v[198:199], v59 offset:128\n"
"ds_read_b64_tr_b16 v[204:205], v60 offset:128\n"
"ds_read_b64_tr_b16 v[206:207], v61 offset:128\n"
"ds_read_b64_tr_b16 v[212:213], v62 offset:128\n"
"ds_read_b64_tr_b16 v[214:215], v63 offset:128\n"
"ds_read_b64_tr_b16 v[220:221], v64 offset:128\n"
"ds_read_b64_tr_b16 v[222:223], v65 offset:128\n"
"ds_read_b64_tr_b16 v[228:229], v58 offset:256\n"
"ds_read_b64_tr_b16 v[230:231], v59 offset:256\n"
"ds_read_b64_tr_b16 v[236:237], v60 offset:256\n"
"ds_read_b64_tr_b16 v[238:239], v61 offset:256\n"
"ds_read_b64_tr_b16 v[244:245], v62 offset:256\n"
"ds_read_b64_tr_b16 v[246:247], v63 offset:256\n"
"ds_read_b64_tr_b16 v[252:253], v64 offset:256\n"
"ds_read_b64_tr_b16 v[254:255], v65 offset:256\n"
"s_waitcnt lgkmcnt(15)\n"
"v_smfmac_f32_16x16x64_bf16 v[8:11], v[70:73], v[160:167], v6\n"
"s_waitcnt lgkmcnt(15)\n"
"v_smfmac_f32_16x16x64_bf16 v[12:15], v[70:73], v[168:175], v6\n"
"s_waitcnt lgkmcnt(15)\n"
"v_smfmac_f32_16x16x64_bf16 v[16:19], v[70:73], v[176:183], v6\n"
"v_add_f32_dpp v20, v8, v9 quad_perm:[0,1,2,3] row_mask:0x1 bank_mask:0xf\n"
"v_mov_b64 v[8:9], v[22:23]\n"
"s_waitcnt lgkmcnt(15)\n"
"v_smfmac_f32_16x16x64_bf16 v[28:31], v[70:73], v[184:191], v6\n"
"v_add_f32_dpp v20, v12, v13 quad_perm:[0,1,2,3] row_mask:0x2 bank_mask:0xf\n"
"v_mov_b64 v[12:13], v[22:23]\n"
"s_waitcnt lgkmcnt(14)\n"
"v_smfmac_f32_16x16x64_bf16 v[8:11], v[70:73], v[192:199], v6\n"
"v_add_f32_dpp v20, v16, v17 quad_perm:[0,1,2,3] row_mask:0x4 bank_mask:0xf\n"
"v_mov_b64 v[16:17], v[22:23]\n"
"s_waitcnt lgkmcnt(12)\n"
"v_smfmac_f32_16x16x64_bf16 v[12:15], v[70:73], v[200:207], v6\n"
"v_add_f32_dpp v20, v28, v29 quad_perm:[0,1,2,3] row_mask:0x8 bank_mask:0xf\n"
"v_mov_b64 v[28:29], v[22:23]\n"
"s_waitcnt lgkmcnt(10)\n"
"v_smfmac_f32_16x16x64_bf16 v[16:19], v[70:73], v[208:215], v6\n"
"v_add_f32_dpp v22, v8, v9 quad_perm:[0,1,2,3] row_mask:0x1 bank_mask:0xf\n"
"v_mov_b64 v[8:9], v[24:25]\n"
"s_waitcnt lgkmcnt(8)\n"
"v_smfmac_f32_16x16x64_bf16 v[28:31], v[70:73], v[216:223], v6\n"
"v_add_f32_dpp v22, v12, v13 quad_perm:[0,1,2,3] row_mask:0x2 bank_mask:0xf\n"
"v_mov_b64 v[12:13], v[24:25]\n"
"s_waitcnt lgkmcnt(6)\n"
"v_smfmac_f32_16x16x64_bf16 v[8:11], v[70:73], v[224:231], v6\n"
"v_add_f32_dpp v22, v16, v17 quad_perm:[0,1,2,3] row_mask:0x4 bank_mask:0xf\n"
"v_mov_b64 v[16:17], v[24:25]\n"
"s_waitcnt lgkmcnt(4)\n"
"v_smfmac_f32_16x16x64_bf16 v[12:15], v[70:73], v[232:239], v6\n"
"v_add_f32_dpp v22, v28, v29 quad_perm:[0,1,2,3] row_mask:0x8 bank_mask:0xf\n"
"v_mov_b64 v[28:29], v[24:25]\n"
"s_waitcnt lgkmcnt(2)\n"
"v_smfmac_f32_16x16x64_bf16 v[16:19], v[70:73], v[240:247], v6\n"
"v_add_f32_dpp v24, v8, v9 quad_perm:[0,1,2,3] row_mask:0x1 bank_mask:0xf\n"
"s_waitcnt lgkmcnt(0)\n"
"v_smfmac_f32_16x16x64_bf16 v[28:31], v[70:73], v[248:255], v6\n"
"v_add_f32_dpp v24, v12, v13 quad_perm:[0,1,2,3] row_mask:0x2 bank_mask:0xf\n"
"s_mul_i32 s70, s48, 768\n"
"v_lshl_add_u32 v33, v0, 2, s70\n"
"v_mul_f32 v20, v20, v76\n"
"ds_write_b32 v33, v20 offset:4096\n"
"v_mul_f32 v22, v22, v76\n"
"ds_write_b32 v33, v22 offset:4352\n"
"v_add_f32_dpp v24, v16, v17 quad_perm:[0,1,2,3] row_mask:0x4 bank_mask:0xf\n"
"s_nop 1\n"
"v_add_f32_dpp v24, v28, v29 quad_perm:[0,1,2,3] row_mask:0x8 bank_mask:0xf\n"
"v_mul_f32 v24, v24, v76\n"
"ds_write_b32 v33, v24 offset:4608\n"
"v_mul_f32 v75, v75, v76\n"
"ds_write_b32 v33, v75 offset:7168\n"
"v_mul_u32_u24 v32, 24, v0\n"
"v_add_u32 v32, 4096, v32\n"
"v_add_u32 v33, 1536, v32\n"
"v_lshlrev_b32 v1, 4, v0\n"
"s_waitcnt lgkmcnt(0)\n"
"s_barrier\n"
"s_cmp_eq_u32 s48, 0\n"
"s_cbranch_scc0 .Lr_64_%=\n"
"ds_read2_b64 v[8:11], v32 offset0:0 offset1:1\n"
"ds_read2_b64 v[12:15], v32 offset0:2 offset1:96\n"
"ds_read2_b64 v[16:19], v32 offset0:97 offset1:98\n"
"ds_read2_b64 v[20:23], v33 offset0:0 offset1:1\n"
"ds_read2_b64 v[24:27], v33 offset0:2 offset1:96\n"
"ds_read2_b64 v[28:31], v33 offset0:97 offset1:98\n"
"s_add_u32 s66, s54, 2\n"
"s_lshl_b32 s71, s50, 9\n"
"s_add_u32 s71, s71, s57\n"
"v_mov_b32 v35, s66\n"
"s_mov_b32 exec_hi, 0\n"
"s_waitcnt lgkmcnt(3)\n"
"v_pk_add_f32 v[8:9], v[8:9], v[14:15]\n"
"v_pk_add_f32 v[10:11], v[10:11], v[16:17]\n"
"v_pk_add_f32 v[12:13], v[12:13], v[18:19]\n"
"s_waitcnt lgkmcnt(0)\n"
"v_pk_add_f32 v[20:21], v[20:21], v[26:27]\n"
"v_pk_add_f32 v[22:23], v[22:23], v[28:29]\n"
"v_pk_add_f32 v[24:25], v[24:25], v[30:31]\n"
"v_pk_add_f32 v[8:9], v[8:9], v[20:21]\n"
"v_pk_add_f32 v[10:11], v[10:11], v[22:23]\n"
"v_pk_add_f32 v[12:13], v[12:13], v[24:25]\n"
"v_cvt_pk_bf16_f32 v32, v8, v9\n"
"v_cvt_pk_bf16_f32 v33, v10, v11\n"
"v_cvt_pk_bf16_f32 v34, v12, v13\n"
"buffer_store_dwordx4 v[32:35], v1, s[40:43], s71 offen offset:0 sc1\n"
"s_mov_b32 exec_hi, -1\n"
".Lr_64_%=:\n"
"s_cmp_eq_u32 s48, 1\n"
"s_cbranch_scc0 .Lr_65_%=\n"
"v_lshlrev_b32 v5, 2, v0\n"
"ds_read2st64_b32 v[8:9], v5 offset0:28 offset1:31\n"
"ds_read2st64_b32 v[10:11], v5 offset0:34 offset1:37\n"
"s_add_u32 s66, s54, 2\n"
"s_lshl_b32 s72, s50, 7\n"
"s_add_u32 s72, s72, s56\n"
"s_add_u32 s72, s72, 6144\n"
"v_mov_b32 v13, s66\n"
"s_waitcnt lgkmcnt(0)\n"
"v_pk_add_f32 v[8:9], v[8:9], v[10:11]\n"
"v_add_f32 v12, v8, v9\n"
"s_nop 1\n"
"v_add_f32_dpp v12, v12, v12 row_ror:1 row_mask:0xf bank_mask:0xf\n"
"s_nop 1\n"
"v_add_f32_dpp v12, v12, v12 row_ror:4 row_mask:0xf bank_mask:0xf\n"
"s_nop 1\n"
"v_add_f32_dpp v12, v12, v12 row_ror:8 row_mask:0xf bank_mask:0xf\n"
"s_nop 1\n"
"v_add_f32_dpp v12, v12, v12 row_bcast:15 row_mask:0xa bank_mask:0xf\n"
"s_nop 1\n"
"v_add_f32_dpp v12, v12, v12 row_bcast:31 row_mask:0xc bank_mask:0xf\n"
"s_mov_b32 exec_lo, 0\n"
"s_brev_b32 exec_hi, 1\n"
"buffer_store_dwordx2 v[12:13], off, s[40:43], s72 offset:1024 sc1\n"
"s_mov_b64 exec, -1\n"
".Lr_65_%=:\n"
"s_cmp_lg_u32 s48, 0\n"
"s_cbranch_scc0 .Lr_66_%=\n"
"s_lshl_b32 s70, s48, 4\n"
"s_sub_u32 s70, s70, 16\n"
"v_lshrrev_b32 v3, 3, v0\n"
"v_add_u32 v3, s70, v3\n"
"v_mul_u32_u24 v4, 43, v3\n"
"v_lshrrev_b32 v4, 7, v4\n"
"v_mad_i32_i24 v3, v4, -3, v3\n"
"v_lshrrev_b32 v1, 1, v4\n"
"v_and_b32 v1, 4, v1\n"
"v_and_or_b32 v1, v4, 2, v1\n"
"v_xor_b32 v1, v0, v1\n"
"v_and_b32 v1, 7, v1\n"
"v_lshl_add_u32 v1, v3, 3, v1\n"
"v_lshlrev_b32 v1, 4, v1\n"
"v_mul_u32_u24 v4, 12288, v4\n"
"v_add_u32 v1, v1, v4\n"
"s_add_u32 s71, s70, 8\n"
"v_lshrrev_b32 v3, 3, v0\n"
"v_add_u32 v3, s71, v3\n"
"v_mul_u32_u24 v4, 43, v3\n"
"v_lshrrev_b32 v4, 7, v4\n"
"v_mad_i32_i24 v3, v4, -3, v3\n"
"v_lshrrev_b32 v2, 1, v4\n"
"v_and_b32 v2, 4, v2\n"
"v_and_or_b32 v2, v4, 2, v2\n"
"v_xor_b32 v2, v0, v2\n"
"v_and_b32 v2, 7, v2\n"
"v_lshl_add_u32 v2, v3, 3, v2\n"
"v_lshlrev_b32 v2, 4, v2\n"
"v_mul_u32_u24 v4, 12288, v4\n"
"v_add_u32 v2, v2, v4\n"
"s_lshl_b32 s70, s70, 7\n"
"s_add_u32 m0, s70, 65536\n"
"s_mov_b32 s71, 0\n"
"buffer_load_dwordx4 v1, s[20:23], s71 offen offset:0 nt lds\n"
"s_add_u32 m0, m0, 1024\n"
"s_nop 0\n"
"buffer_load_dwordx4 v2, s[20:23], s71 offen offset:0 nt lds\n"
".Lr_66_%=:\n"
"s_mul_i32 s75, s49, 160\n"
"s_add_u32 s75, s75, s58\n"
"s_lshl_b32 s78, s48, 5\n"
"s_add_u32 s75, s75, s78\n"
"s_mov_b32 s78, 0x55555555\n"
"s_cmp_eq_u32 s48, 3\n"
"s_cselect_b32 s79, s78, 0\n"
"s_cmp_eq_u32 s48, 0\n"
"s_cbranch_scc0 .Lr_67_%=\n"
"v_and_b32 v31, 48, v0\n"
"v_lshlrev_b32 v31, 3, v31\n"
"v_add_u32 v6, 6144, v31\n"
"buffer_load_dwordx2 v[24:25], v6, s[40:43], s56 offen offset:0 sc1\n"
"buffer_load_dwordx2 v[28:29], v6, s[40:43], s56 offen offset:512 sc1\n"
"v_and_b32 v36, 15, v0\n"
"v_lshlrev_b32 v36, 1, v36\n"
"s_mov_b32 s70, 0x4e44e4\n"
"v_bfe_u32 v1, s70, v36, 2\n"
"s_mov_b32 s70, 0xfaa500\n"
"v_bfe_u32 v36, s70, v36, 2\n"
"v_mul_u32_u24 v1, 5, v1\n"
"v_lshlrev_b32 v2, 2, v36\n"
"v_lshlrev_b32 v1, v2, v1\n"
"v_lshlrev_b32 v36, v36, 1\n"
"v_bfe_i32 v2, v36, 0, 1\n"
"v_bfe_i32 v3, v36, 1, 1\n"
"v_bfe_i32 v4, v36, 2, 1\n"
"v_bfe_i32 v5, v36, 3, 1\n"
"v_and_b32 v7, 15, v0\n"
"v_bfe_u32 v36, v0, 4, 1\n"
"v_lshl_add_u32 v7, v7, 1, v36\n"
"v_and_b32 v36, 32, v0\n"
"v_add_lshl_u32 v7, v7, v36, 4\n"
"v_mov_b64 v[32:33], 0\n"
"v_mov_b64 v[34:35], 0\n"
".Lr_67_%=:\n"
"s_cmp_eq_u32 s48, 0\n"
"s_cbranch_scc0 .Lr_68_%=\n"
"s_sleep 26\n"
"buffer_load_dwordx4 v[8:11], v7, s[40:43], s57 offen offset:0 sc1\n"
"buffer_load_dwordx4 v[12:15], v7, s[40:43], s57 offen offset:1024 sc1\n"
"buffer_load_dwordx4 v[16:19], v7, s[40:43], s57 offen offset:2048 sc1\n"
"buffer_load_dwordx4 v[20:23], v7, s[40:43], s57 offen offset:3072 sc1\n"
"buffer_load_dwordx2 v[26:27], v6, s[40:43], s56 offen offset:1024 sc1\n"
"buffer_load_dwordx2 v[30:31], v6, s[40:43], s56 offen offset:1536 sc1\n"
".Lr_68_%=:\n"
"s_barrier\n"
"s_cmp_lg_u32 s48, 0\n"
"s_cbranch_scc0 .Lr_69_%=\n"
"s_mov_b32 m0, 768\n"
"s_mul_i32 s70, s48, 32\n"
"s_add_u32 s70, s70, 64\n"
"v_add_u32 v2, s70, v0\n"
"v_lshlrev_b32 v2, 7, v2\n"
"s_mul_i32 s70, s52, 24576\n"
"s_mov_b32 exec_lo, 0xffffffff\n"
"s_mov_b32 exec_hi, 0x0\n"
"buffer_load_dword v2, s[28:31], s70 offen lds\n"
"s_mov_b64 exec, -1\n"
".Lr_69_%=:\n"
"v_lshrrev_b32 v93, 2, v0\n"
"v_and_b32 v92, 12, v93\n"
"v_add_u32 v93, v93, v92\n"
"v_mul_u32_u24 v93, 20, v93\n"
"v_and_b32 v92, 3, v0\n"
"v_add_lshl_u32 v92, v92, v93, 3\n"
"s_lshl_b32 s70, s48, 5\n"
"s_add_u32 s70, s70, 126976\n"
"v_add_u32 v92, s70, v92\n"
"s_cmp_lg_u32 s48, 0\n"
"s_cbranch_scc0 .Lr_70_%=\n"
"s_waitcnt vmcnt(4)\n"
"s_bitcmp1_b32 12, s48\n"
"s_cbranch_scc0 .Lr_71_%=\n"
"s_waitcnt vmcnt(3)\n"
".Lr_71_%=:\n"
".Lr_70_%=:\n"
"s_barrier\n"
"ds_read_b64_tr_b16 v[44:45], v92 offset:0\n"
"ds_read_b64_tr_b16 v[46:47], v92 offset:640\n"
"ds_read_b64_tr_b16 v[48:49], v92 offset:5120\n"
"ds_read_b64_tr_b16 v[50:51], v92 offset:5760\n"
"ds_read_b64_tr_b16 v[52:53], v92 offset:12288\n"
"ds_read_b64_tr_b16 v[54:55], v92 offset:12928\n"
"ds_read_b64_tr_b16 v[56:57], v92 offset:17408\n"
"ds_read_b64_tr_b16 v[58:59], v92 offset:18048\n"
"ds_read_b64_tr_b16 v[60:61], v92 offset:24576\n"
"ds_read_b64_tr_b16 v[62:63], v92 offset:25216\n"
"ds_read_b64_tr_b16 v[64:65], v92 offset:29696\n"
"ds_read_b64_tr_b16 v[66:67], v92 offset:30336\n"
"s_cmp_eq_u32 s48, 3\n"
"s_cbranch_scc0 .Lr_72_%=\n"
"ds_read_b64_tr_b16 v[68:69], v92 offset:32\n"
"ds_read_b64_tr_b16 v[70:71], v92 offset:672\n"
"ds_read_b64_tr_b16 v[72:73], v92 offset:5152\n"
"ds_read_b64_tr_b16 v[74:75], v92 offset:5792\n"
"ds_read_b64_tr_b16 v[76:77], v92 offset:12320\n"
"ds_read_b64_tr_b16 v[78:79], v92 offset:12960\n"
"ds_read_b64_tr_b16 v[80:81], v92 offset:17440\n"
"ds_read_b64_tr_b16 v[82:83], v92 offset:18080\n"
"ds_read_b64_tr_b16 v[84:85], v92 offset:24608\n"
"ds_read_b64_tr_b16 v[86:87], v92 offset:25248\n"
"ds_read_b64_tr_b16 v[88:89], v92 offset:29728\n"
"ds_read_b64_tr_b16 v[90:91], v92 offset:30368\n"
"v_lshlrev_b32 v7, 4, v0\n"
"s_waitcnt lgkmcnt(0)\n"
"ds_write_b128 v7, v[68:71] offset:4096\n"
"ds_write_b128 v7, v[72:75] offset:5120\n"
"ds_write_b128 v7, v[76:79] offset:6144\n"
"ds_write_b128 v7, v[80:83] offset:7168\n"
"ds_write_b128 v7, v[84:87] offset:8192\n"
"ds_write_b128 v7, v[88:91] offset:9216\n"
".Lr_72_%=:\n"
"s_cmp_lg_u32 s48, 0\n"
"s_cbranch_scc0 .Lr_73_%=\n"
"v_lshrrev_b32 v42, 4, v0\n"
"v_and_b32 v38, 1, v0\n"
"v_lshl_add_u32 v42, v42, 1, v38\n"
"v_lshlrev_b32 v42, 4, v42\n"
".Lr_73_%=:\n"
"s_cmp_eq_u32 s48, 0\n"
"s_cbranch_scc0 .Lr_74_%=\n"
".Lgb_75_%=:\n"
"s_waitcnt vmcnt(6)\n"
"v_max_f32 v36, v24, v28\n"
"s_nop 1\n"
"v_max_f32_dpp v36, v36, v36 row_bcast:15 row_mask:0xa bank_mask:0xf\n"
"s_nop 1\n"
"v_max_f32_dpp v36, v36, v36 row_bcast:31 row_mask:0xc bank_mask:0xf\n"
"s_nop 0\n"
"v_readlane_b32 s70, v36, 63\n"
"v_bitop3_b32 v29, v25, v29, s66 bitop3:0x7e\n"
"s_nop 0\n"
"v_subrev_f32 v24, s70, v24\n"
"v_subrev_f32 v28, s70, v28\n"
"v_exp_f32 v24, v24\n"
"v_exp_f32 v28, v28\n"
"v_cvt_pk_bf16_f32 v24, v24, 0\n"
"v_cvt_pk_bf16_f32 v28, v28, 0\n"
"v_and_b32 v36, 15, v0\n"
"v_lshrrev_b32 v25, 4, v0\n"
"v_mad_u32_u24 v36, v36, 3, v25\n"
"v_and_b32 v25, 60, v36\n"
"v_add_lshl_u32 v25, v25, v36, 2\n"
"v_and_b32 v36, v2, v24\n"
"v_and_b32 v37, v3, v24\n"
"v_and_b32 v38, v4, v24\n"
"v_and_b32 v39, v5, v24\n"
"v_and_b32 v40, v2, v28\n"
"v_and_b32 v41, v3, v28\n"
"v_and_b32 v42, v4, v28\n"
"v_and_b32 v43, v5, v28\n"
"v_lshlrev_b32 v24, 16, v24\n"
"v_lshlrev_b32 v28, 16, v28\n"
"s_waitcnt vmcnt(4)\n"
"v_smfmac_f32_16x16x64_bf16 v[32:35], v[36:39], v[8:15], v1\n"
"s_waitcnt vmcnt(2)\n"
"v_smfmac_f32_16x16x64_bf16 v[32:35], v[40:43], v[16:23], v1\n"
"s_waitcnt vmcnt(0)\n"
"v_mul_f32 v24, v24, v26\n"
"v_fmac_f32 v24, v28, v30\n"
"v_bitop3_b32 v29, v11, v29, s66 bitop3:0xde\n"
"v_bitop3_b32 v29, v15, v29, s66 bitop3:0xde\n"
"v_add_f32_dpp v24, v24, v24 row_bcast:15 row_mask:0xa bank_mask:0xf\n"
"v_bitop3_b32 v29, v19, v29, s66 bitop3:0xde\n"
"v_bitop3_b32 v29, v23, v29, s66 bitop3:0xde\n"
"v_add_f32_dpp v24, v24, v24 row_bcast:31 row_mask:0xc bank_mask:0xf\n"
"v_bitop3_b32 v29, v27, v29, s66 bitop3:0xde\n"
"v_rcp_f32 v24, v24\n"
"v_bitop3_b32 v29, v31, v29, s66 bitop3:0xde\n"
"v_readlane_b32 s70, v24, 63\n"
"v_cmp_ne_u32 vcc, 0, v29\n"
"s_mov_b32 exec_hi, 0xffff\n"
"v_mul_f32 v32, s70, v32\n"
"v_mul_f32 v33, s70, v33\n"
"v_mul_f32 v34, s70, v34\n"
"v_mul_f32 v35, s70, v35\n"
"v_cvt_pk_bf16_f32 v36, v32, v33\n"
"v_cvt_pk_bf16_f32 v37, v34, v35\n"
"ds_write2_b32 v25, v36, v37 offset0:96 offset1:100\n"
"s_mov_b32 exec_hi, -1\n"
"s_cbranch_vccnz .Lgr_76_%=\n"
".Lgo_77_%=:\n"
"v_lshrrev_b32 v42, 4, v0\n"
"v_and_b32 v36, 1, v0\n"
"v_lshl_add_u32 v42, v42, 1, v36\n"
"v_lshlrev_b32 v42, 4, v42\n"
".Lr_74_%=:\n"
"s_waitcnt lgkmcnt(0)\n"
"s_barrier\n"
"ds_read_b128 v[32:35], v42 offset:384\n"
"ds_read_b128 v[36:39], v42 offset:512\n"
"ds_read_b128 v[40:43], v42 offset:640\n"
"v_and_b32 v6, 1, v0\n"
"v_mul_u32_u24 v6, 0xaaaa, v6\n"
"v_add_u32 v6, 0x4444, v6\n"
"v_mov_b64 v[8:9], 0\n"
"v_mov_b64 v[2:3], 0\n"
"v_and_b32 v1, 15, v0\n"
"v_and_b32 v4, 32, v0\n"
"v_lshl_add_u32 v1, v1, 1, v4\n"
"v_and_b32 v4, 16, v0\n"
"v_lshl_add_u32 v1, v4, 9, v1\n"
"s_waitcnt lgkmcnt(2)\n"
"v_smfmac_f32_16x16x64_bf16 v[2:5], v[32:35], v[44:51], v6\n"
"v_smfmac_f32_16x16x64_bf16 v[8:11], v[32:35], v[68:75], v6\n"
"s_waitcnt lgkmcnt(1)\n"
"v_smfmac_f32_16x16x64_bf16 v[2:5], v[36:39], v[52:59], v6\n"
"v_smfmac_f32_16x16x64_bf16 v[8:11], v[36:39], v[76:83], v6\n"
"s_waitcnt lgkmcnt(0)\n"
"v_smfmac_f32_16x16x64_bf16 v[2:5], v[40:43], v[60:67], v6\n"
"v_smfmac_f32_16x16x64_bf16 v[8:11], v[40:43], v[84:91], v6\n"
"s_nop 3\n"
"v_add_f32 v12, v2, v3\n"
"s_nop 2\n"
"v_add_f32_dpp v12, v8, v9 quad_perm:[0,1,2,3] row_mask:0xc bank_mask:0xf\n"
"s_nop 1\n"
"v_mov_b32_dpp v2, v12 quad_perm:[1,1,3,3] row_mask:0xf bank_mask:0xf\n"
"s_mov_b64 exec, s[78:79]\n"
"v_cvt_pk_bf16_f32 v2, v12, v2\n"
"buffer_store_dword v2, v1, s[40:43], s75 offen offset:0 sc1\n"
"s_mov_b64 exec, -1\n"
"s_barrier\n"
"s_cmp_lg_u32 s48, 0\n"
"s_cbranch_scc0 .Lr_78_%=\n"
"s_lshl_b32 s70, s48, 4\n"
"s_sub_u32 s70, s70, 16\n"
"v_lshrrev_b32 v3, 3, v0\n"
"v_add_u32 v3, s70, v3\n"
"v_mul_u32_u24 v4, 43, v3\n"
"v_lshrrev_b32 v4, 7, v4\n"
"v_mad_i32_i24 v3, v4, -3, v3\n"
"v_lshrrev_b32 v1, 1, v4\n"
"v_and_b32 v1, 4, v1\n"
"v_and_or_b32 v1, v4, 2, v1\n"
"v_xor_b32 v1, v0, v1\n"
"v_and_b32 v1, 7, v1\n"
"v_lshl_add_u32 v1, v3, 3, v1\n"
"v_lshlrev_b32 v1, 4, v1\n"
"v_mul_u32_u24 v4, 12288, v4\n"
"v_add_u32 v1, v1, v4\n"
"s_add_u32 s71, s70, 8\n"
"v_lshrrev_b32 v3, 3, v0\n"
"v_add_u32 v3, s71, v3\n"
"v_mul_u32_u24 v4, 43, v3\n"
"v_lshrrev_b32 v4, 7, v4\n"
"v_mad_i32_i24 v3, v4, -3, v3\n"
"v_lshrrev_b32 v2, 1, v4\n"
"v_and_b32 v2, 4, v2\n"
"v_and_or_b32 v2, v4, 2, v2\n"
"v_xor_b32 v2, v0, v2\n"
"v_and_b32 v2, 7, v2\n"
"v_lshl_add_u32 v2, v3, 3, v2\n"
"v_lshlrev_b32 v2, 4, v2\n"
"v_mul_u32_u24 v4, 12288, v4\n"
"v_add_u32 v2, v2, v4\n"
"s_lshl_b32 s70, s70, 7\n"
"s_add_u32 m0, s70, 71680\n"
"s_mov_b32 s71, 196608\n"
"buffer_load_dwordx4 v1, s[20:23], s71 offen offset:0 nt lds\n"
"s_add_u32 m0, m0, 1024\n"
"s_nop 0\n"
"buffer_load_dwordx4 v2, s[20:23], s71 offen offset:0 nt lds\n"
"s_add_u32 m0, s70, 77824\n"
"s_mov_b32 s71, 393216\n"
"buffer_load_dwordx4 v1, s[20:23], s71 offen offset:0 nt lds\n"
"s_add_u32 m0, m0, 1024\n"
"s_nop 0\n"
"buffer_load_dwordx4 v2, s[20:23], s71 offen offset:0 nt lds\n"
"s_add_u32 m0, s70, 83968\n"
"s_mov_b32 s71, 589824\n"
"buffer_load_dwordx4 v1, s[20:23], s71 offen offset:0 nt lds\n"
"s_add_u32 m0, m0, 1024\n"
"s_nop 0\n"
"buffer_load_dwordx4 v2, s[20:23], s71 offen offset:0 nt lds\n"
"s_add_u32 m0, s70, 90112\n"
"s_mov_b32 s71, 786432\n"
"buffer_load_dwordx4 v1, s[20:23], s71 offen offset:0 nt lds\n"
"s_add_u32 m0, m0, 1024\n"
"s_nop 0\n"
"buffer_load_dwordx4 v2, s[20:23], s71 offen offset:0 nt lds\n"
"s_lshl_b32 s70, s48, 4\n"
"s_sub_u32 s70, s70, 16\n"
"v_lshrrev_b32 v3, 3, v0\n"
"v_add_u32 v3, s70, v3\n"
"v_mul_u32_u24 v4, 43, v3\n"
"v_lshrrev_b32 v4, 7, v4\n"
"v_mad_i32_i24 v3, v4, -3, v3\n"
"v_lshrrev_b32 v1, 1, v4\n"
"v_xor_b32 v1, v0, v1\n"
"v_and_b32 v1, 7, v1\n"
"v_lshl_add_u32 v1, v3, 3, v1\n"
"v_lshlrev_b32 v1, 4, v1\n"
"v_and_b32 v3, 3, v4\n"
"v_lshlrev_b32 v3, 2, v3\n"
"v_lshrrev_b32 v4, 1, v4\n"
"v_and_or_b32 v3, v4, 2, v3\n"
"v_and_b32 v4, 4, v4\n"
"v_lshl_add_u32 v4, v4, 2, v3\n"
"v_mul_u32_u24 v4, 12288, v4\n"
"v_add_u32 v1, v1, v4\n"
"s_add_u32 s71, s70, 8\n"
"v_lshrrev_b32 v3, 3, v0\n"
"v_add_u32 v3, s71, v3\n"
"v_mul_u32_u24 v4, 43, v3\n"
"v_lshrrev_b32 v4, 7, v4\n"
"v_mad_i32_i24 v3, v4, -3, v3\n"
"v_lshrrev_b32 v2, 1, v4\n"
"v_xor_b32 v2, v0, v2\n"
"v_and_b32 v2, 7, v2\n"
"v_lshl_add_u32 v2, v3, 3, v2\n"
"v_lshlrev_b32 v2, 4, v2\n"
"v_and_b32 v3, 3, v4\n"
"v_lshlrev_b32 v3, 2, v3\n"
"v_lshrrev_b32 v4, 1, v4\n"
"v_and_or_b32 v3, v4, 2, v3\n"
"v_and_b32 v4, 4, v4\n"
"v_lshl_add_u32 v4, v4, 2, v3\n"
"v_mul_u32_u24 v4, 12288, v4\n"
"v_add_u32 v2, v2, v4\n"
"s_lshl_b32 s70, s70, 7\n"
"s_add_u32 m0, s70, 10240\n"
"s_mov_b32 s71, 0\n"
"buffer_load_dwordx4 v1, s[24:27], s71 offen offset:0 nt lds\n"
"s_add_u32 m0, m0, 1024\n"
"s_nop 0\n"
"buffer_load_dwordx4 v2, s[24:27], s71 offen offset:0 nt lds\n"
"s_add_u32 m0, s70, 16384\n"
"s_mov_b32 s71, 12288\n"
"buffer_load_dwordx4 v1, s[24:27], s71 offen offset:0 nt lds\n"
"s_add_u32 m0, m0, 1024\n"
"s_nop 0\n"
"buffer_load_dwordx4 v2, s[24:27], s71 offen offset:0 nt lds\n"
"s_add_u32 m0, s70, 22528\n"
"s_mov_b32 s71, 393216\n"
"buffer_load_dwordx4 v1, s[24:27], s71 offen offset:0 nt lds\n"
"s_add_u32 m0, m0, 1024\n"
"s_nop 0\n"
"buffer_load_dwordx4 v2, s[24:27], s71 offen offset:0 nt lds\n"
"s_add_u32 m0, s70, 28672\n"
"s_mov_b32 s71, 405504\n"
"buffer_load_dwordx4 v1, s[24:27], s71 offen offset:0 nt lds\n"
"s_add_u32 m0, m0, 1024\n"
"s_nop 0\n"
"buffer_load_dwordx4 v2, s[24:27], s71 offen offset:0 nt lds\n"
".Lr_78_%=:\n"
"v_lshrrev_b32 v70, 2, v0\n"
"v_and_b32 v71, 12, v70\n"
"v_add_u32 v70, v70, v71\n"
"v_lshrrev_b32 v71, 1, v70\n"
"v_and_b32 v71, 4, v71\n"
"v_and_or_b32 v71, v70, 2, v71\n"
"v_lshlrev_b32 v71, 1, v71\n"
"v_and_or_b32 v71, v0, 3, v71\n"
"v_lshlrev_b32 v71, 3, v71\n"
"v_mul_u32_u24 v70, 384, v70\n"
"v_add_u32 v68, v70, v71\n"
"v_lshrrev_b32 v70, 2, v0\n"
"v_lshrrev_b32 v71, 1, v70\n"
"v_and_b32 v71, 4, v71\n"
"v_and_or_b32 v71, v70, 2, v71\n"
"v_lshlrev_b32 v71, 1, v71\n"
"v_and_or_b32 v71, v0, 3, v71\n"
"v_lshlrev_b32 v71, 3, v71\n"
"v_mul_u32_u24 v70, 384, v70\n"
"v_add_u32 v69, v70, v71\n"
"s_mul_i32 s70, s48, 3\n"
"s_add_u32 s70, s70, 0\n"
"s_and_b32 s71, s70, 3\n"
"s_lshl_b32 s71, s71, 5\n"
"s_lshr_b32 s70, s70, 2\n"
"s_lshl_b32 s70, s70, 7\n"
"s_add_u32 s70, s70, 65536\n"
"v_xor_b32 v74, s71, v68\n"
"v_add_u32 v74, s70, v74\n"
"s_mul_i32 s70, s48, 3\n"
"s_add_u32 s70, s70, 0\n"
"s_and_b32 s71, s70, 3\n"
"s_lshl_b32 s71, s71, 5\n"
"s_lshr_b32 s70, s70, 2\n"
"s_lshl_b32 s70, s70, 7\n"
"s_add_u32 s70, s70, 65536\n"
"v_xor_b32 v76, s71, v69\n"
"v_add_u32 v76, s70, v76\n"
"s_mul_i32 s70, s48, 3\n"
"s_add_u32 s70, s70, 1\n"
"s_and_b32 s71, s70, 3\n"
"s_lshl_b32 s71, s71, 5\n"
"s_lshr_b32 s70, s70, 2\n"
"s_lshl_b32 s70, s70, 7\n"
"s_add_u32 s70, s70, 65536\n"
"v_xor_b32 v84, s71, v68\n"
"v_add_u32 v84, s70, v84\n"
"s_mul_i32 s70, s48, 3\n"
"s_add_u32 s70, s70, 1\n"
"s_and_b32 s71, s70, 3\n"
"s_lshl_b32 s71, s71, 5\n"
"s_lshr_b32 s70, s70, 2\n"
"s_lshl_b32 s70, s70, 7\n"
"s_add_u32 s70, s70, 65536\n"
"v_xor_b32 v86, s71, v69\n"
"v_add_u32 v86, s70, v86\n"
"s_mul_i32 s70, s48, 3\n"
"s_add_u32 s70, s70, 2\n"
"s_and_b32 s71, s70, 3\n"
"s_lshl_b32 s71, s71, 5\n"
"s_lshr_b32 s70, s70, 2\n"
"s_lshl_b32 s70, s70, 7\n"
"s_add_u32 s70, s70, 65536\n"
"v_xor_b32 v94, s71, v68\n"
"v_add_u32 v94, s70, v94\n"
"s_mul_i32 s70, s48, 3\n"
"s_add_u32 s70, s70, 2\n"
"s_and_b32 s71, s70, 3\n"
"s_lshl_b32 s71, s71, 5\n"
"s_lshr_b32 s70, s70, 2\n"
"s_lshl_b32 s70, s70, 7\n"
"s_add_u32 s70, s70, 65536\n"
"v_xor_b32 v96, s71, v69\n"
"v_add_u32 v96, s70, v96\n"
"s_add_u32 s66, s54, 3\n"
"s_mov_b32 s78, 0x55555555\n"
"s_cmp_eq_u32 s48, 3\n"
"s_cselect_b32 s79, 3, 0\n"
"s_cmp_eq_u32 s48, 0\n"
"s_cbranch_scc0 .Lr_79_%=\n"
"v_and_b32 v8, 15, v0\n"
"v_lshrrev_b32 v9, 4, v0\n"
"v_lshl_add_u32 v8, v8, 1, v9\n"
"v_lshlrev_b32 v8, 3, v8\n"
"v_lshlrev_b32 v8, 1, v8\n"
"ds_read_b128 v[40:43], v8 offset:2560\n"
"v_and_b32 v1, 3, v0\n"
"v_mul_u32_u24 v1, 0x5555, v1\n"
"v_bfe_i32 v3, v0, 2, 1\n"
"v_and_b32 v3, 0x3f80, v3\n"
"v_xor_b32 v2, 0x3f80, v3\n"
"v_mov_b64 v[4:5], v[2:3]\n"
"v_and_b32 v7, 15, v0\n"
"v_min_u32 v7, 9, v7\n"
"v_mul_u32_u24 v9, 160, v9\n"
"v_lshl_add_u32 v7, v7, 4, v9\n"
"s_add_u32 s73, s58, s59\n"
"s_add_u32 s74, s73, 2560\n"
".Lr_79_%=:\n"
"s_cmp_eq_u32 s48, 0\n"
"s_cbranch_scc0 .Lr_80_%=\n"
"s_sleep 18\n"
"buffer_load_dwordx4 v[8:11], v7, s[40:43], s73 offen offset:0 sc1\n"
"buffer_load_dwordx4 v[12:15], v7, s[40:43], s73 offen offset:640 sc1\n"
"buffer_load_dwordx4 v[16:19], v7, s[40:43], s73 offen offset:1280 sc1\n"
"buffer_load_dwordx4 v[20:23], v7, s[40:43], s73 offen offset:1920 sc1\n"
"buffer_load_dwordx4 v[24:27], v7, s[40:43], s74 offen offset:0 sc1\n"
"buffer_load_dwordx4 v[28:31], v7, s[40:43], s74 offen offset:640 sc1\n"
"buffer_load_dwordx4 v[32:35], v7, s[40:43], s74 offen offset:1280 sc1\n"
"buffer_load_dwordx4 v[36:39], v7, s[40:43], s74 offen offset:1920 sc1\n"
".Lr_80_%=:\n"
"s_barrier\n"
"s_cmp_lg_u32 s48, 0\n"
"s_cbranch_scc0 .Lr_81_%=\n"
"s_lshl_b32 s70, s48, 4\n"
"s_sub_u32 s70, s70, 16\n"
"v_lshrrev_b32 v3, 3, v0\n"
"v_add_u32 v3, s70, v3\n"
"v_mul_u32_u24 v4, 43, v3\n"
"v_lshrrev_b32 v4, 7, v4\n"
"v_mad_i32_i24 v3, v4, -3, v3\n"
"v_lshrrev_b32 v1, 1, v4\n"
"v_xor_b32 v1, v0, v1\n"
"v_and_b32 v1, 7, v1\n"
"v_lshl_add_u32 v1, v3, 3, v1\n"
"v_lshlrev_b32 v1, 4, v1\n"
"v_and_b32 v3, 3, v4\n"
"v_lshlrev_b32 v3, 2, v3\n"
"v_lshrrev_b32 v4, 1, v4\n"
"v_and_or_b32 v3, v4, 2, v3\n"
"v_and_b32 v4, 4, v4\n"
"v_lshl_add_u32 v4, v4, 2, v3\n"
"v_mul_u32_u24 v4, 12288, v4\n"
"v_add_u32 v1, v1, v4\n"
"s_add_u32 s71, s70, 8\n"
"v_lshrrev_b32 v3, 3, v0\n"
"v_add_u32 v3, s71, v3\n"
"v_mul_u32_u24 v4, 43, v3\n"
"v_lshrrev_b32 v4, 7, v4\n"
"v_mad_i32_i24 v3, v4, -3, v3\n"
"v_lshrrev_b32 v2, 1, v4\n"
"v_xor_b32 v2, v0, v2\n"
"v_and_b32 v2, 7, v2\n"
"v_lshl_add_u32 v2, v3, 3, v2\n"
"v_lshlrev_b32 v2, 4, v2\n"
"v_and_b32 v3, 3, v4\n"
"v_lshlrev_b32 v3, 2, v3\n"
"v_lshrrev_b32 v4, 1, v4\n"
"v_and_or_b32 v3, v4, 2, v3\n"
"v_and_b32 v4, 4, v4\n"
"v_lshl_add_u32 v4, v4, 2, v3\n"
"v_mul_u32_u24 v4, 12288, v4\n"
"v_add_u32 v2, v2, v4\n"
"s_lshl_b32 s70, s70, 7\n"
"s_add_u32 m0, s70, 34816\n"
"s_mov_b32 s71, 786432\n"
"buffer_load_dwordx4 v1, s[24:27], s71 offen offset:0 nt lds\n"
"s_add_u32 m0, m0, 1024\n"
"s_nop 0\n"
"buffer_load_dwordx4 v2, s[24:27], s71 offen offset:0 nt lds\n"
"s_add_u32 m0, s70, 40960\n"
"s_mov_b32 s71, 798720\n"
"buffer_load_dwordx4 v1, s[24:27], s71 offen offset:0 nt lds\n"
"s_add_u32 m0, m0, 1024\n"
"s_nop 0\n"
"buffer_load_dwordx4 v2, s[24:27], s71 offen offset:0 nt lds\n"
".Lr_81_%=:\n"
"s_cmp_lg_u32 s48, 0\n"
"s_cbranch_scc0 .Lr_82_%=\n"
"s_waitcnt vmcnt(12)\n"
".Lr_82_%=:\n"
"s_barrier\n"
"ds_read_b64_tr_b16 v[68:69], v74 offset:0\n"
"ds_read_b64_tr_b16 v[70:71], v74 offset:1536\n"
"ds_read_b64_tr_b16 v[72:73], v74 offset:12288\n"
"ds_read_b64_tr_b16 v[74:75], v74 offset:13824\n"
"ds_read_b64_tr_b16 v[76:77], v76 offset:24576\n"
"ds_read_b64_tr_b16 v[78:79], v84 offset:0\n"
"ds_read_b64_tr_b16 v[80:81], v84 offset:1536\n"
"ds_read_b64_tr_b16 v[82:83], v84 offset:12288\n"
"ds_read_b64_tr_b16 v[84:85], v84 offset:13824\n"
"ds_read_b64_tr_b16 v[86:87], v86 offset:24576\n"
"ds_read_b64_tr_b16 v[88:89], v94 offset:0\n"
"ds_read_b64_tr_b16 v[90:91], v94 offset:1536\n"
"ds_read_b64_tr_b16 v[92:93], v94 offset:12288\n"
"ds_read_b64_tr_b16 v[94:95], v94 offset:13824\n"
"ds_read_b64_tr_b16 v[96:97], v96 offset:24576\n"
"s_cmp_lg_u32 s48, 0\n"
"s_cbranch_scc0 .Lr_83_%=\n"
"v_and_b32 v2, 1, v0\n"
"v_mul_u32_u24 v2, 160, v2\n"
"v_lshrrev_b32 v3, 4, v0\n"
"v_lshl_add_u32 v1, v3, 4, v2\n"
"v_lshl_add_u32 v2, v3, 3, v2\n"
"s_waitcnt lgkmcnt(0)\n"
".Lr_83_%=:\n"
"s_cmp_eq_u32 s48, 0\n"
"s_cbranch_scc0 .Lr_84_%=\n"
".Lgb_85_%=:\n"
"s_waitcnt lgkmcnt(0)\n"
"s_waitcnt vmcnt(6)\n"
"v_smfmac_f32_16x16x64_bf16 v[40:43], v[2:5], v[8:15], v1\n"
"v_max_u32 v6, v8, v9\n"
"v_max3_u32 v6, v6, v10, v11\n"
"v_max3_u32 v6, v6, v12, v13\n"
"v_max3_u32 v6, v6, v14, v15\n"
"s_waitcnt vmcnt(4)\n"
"v_smfmac_f32_16x16x64_bf16 v[40:43], v[2:5], v[16:23], v1\n"
"v_max3_u32 v6, v6, v16, v17\n"
"v_max3_u32 v6, v6, v18, v19\n"
"v_max3_u32 v6, v6, v20, v21\n"
"v_max3_u32 v6, v6, v22, v23\n"
"v_and_b32 v8, 15, v0\n"
"v_lshrrev_b32 v9, 4, v0\n"
"v_lshl_add_u32 v8, v8, 1, v9\n"
"v_lshlrev_b32 v8, 3, v8\n"
"s_waitcnt vmcnt(2)\n"
"v_smfmac_f32_16x16x64_bf16 v[40:43], v[2:5], v[24:31], v1\n"
"v_max3_u32 v6, v6, v24, v25\n"
"v_max3_u32 v6, v6, v26, v27\n"
"v_max3_u32 v6, v6, v28, v29\n"
"v_max3_u32 v6, v6, v30, v31\n"
"s_waitcnt vmcnt(0)\n"
"v_smfmac_f32_16x16x64_bf16 v[40:43], v[2:5], v[32:39], v1\n"
"v_max3_u32 v6, v6, v32, v33\n"
"v_max3_u32 v6, v6, v34, v35\n"
"v_max3_u32 v6, v6, v36, v37\n"
"v_max3_u32 v6, v6, v38, v39\n"
"v_cmp_eq_u32 vcc, -1, v6\n"
"s_mov_b64 exec, 0x03ff03ff\n"
"s_nop 1\n"
"v_cvt_pk_bf16_f32 v10, v40, v41\n"
"v_cvt_pk_bf16_f32 v11, v42, v43\n"
"ds_write_b64 v8, v[10:11] offset:0\n"
"s_mov_b64 exec, -1\n"
"s_cbranch_vccnz .Lgr_86_%=\n"
".Lgo_87_%=:\n"
"v_and_b32 v2, 1, v0\n"
"v_mul_u32_u24 v2, 160, v2\n"
"v_lshrrev_b32 v3, 4, v0\n"
"v_lshl_add_u32 v1, v3, 4, v2\n"
"v_lshl_add_u32 v2, v3, 3, v2\n"
"s_waitcnt lgkmcnt(0)\n"
".Lr_84_%=:\n"
"s_barrier\n"
"ds_read_b128 v[18:21], v1 offset:0\n"
"ds_read_b128 v[22:25], v1 offset:64\n"
"ds_read_b64 v[26:27], v2 offset:128\n"
"s_cmp_eq_u32 s48, 0\n"
"s_cbranch_scc0 .Lr_88_%=\n"
"v_lshlrev_b32 v12, 1, v8\n"
"s_mov_b64 exec, 0x03ff03ff\n"
"ds_write_b128 v12, v[40:43] offset:2560\n"
"s_mov_b64 exec, -1\n"
".Lr_88_%=:\n"
"s_cmp_lg_u32 s48, 0\n"
"s_cbranch_scc0 .Lr_89_%=\n"
"v_lshl_add_u32 v3, s48, 5, v0\n"
"v_subrev_u32 v3, 32, v3\n"
"v_lshrrev_b32 v3, 6, v3\n"
"v_xor_b32 v3, v0, v3\n"
"v_lshlrev_b32 v3, 4, v3\n"
"s_lshl_b32 s70, s48, 10\n"
"s_add_u32 s70, s70, 48128\n"
"s_add_u32 m0, s70, 65536\n"
"v_xor_b32 v4, 0, v3\n"
"buffer_load_dwordx4 v4, s[28:31], s70 offen offset:0 lds\n"
"s_add_u32 s70, s70, 3072\n"
"s_add_u32 m0, s70, 65536\n"
"v_xor_b32 v4, 32, v3\n"
"buffer_load_dwordx4 v4, s[28:31], s70 offen offset:0 lds\n"
"s_add_u32 s70, s70, 3072\n"
"s_add_u32 m0, s70, 65536\n"
"v_xor_b32 v4, 64, v3\n"
"buffer_load_dwordx4 v4, s[28:31], s70 offen offset:0 lds\n"
"s_add_u32 s70, s70, 3072\n"
"s_add_u32 m0, s70, 65536\n"
"v_xor_b32 v4, 96, v3\n"
"buffer_load_dwordx4 v4, s[28:31], s70 offen offset:0 lds\n"
".Lr_89_%=:\n"
"s_lshl2_add_u32 s70, s50, s48\n"
"s_lshl_b32 s70, s70, 7\n"
"v_lshl_add_u32 v16, v0, 3, s70\n"
"v_add_u32 v17, 266112, v16\n"
"s_nop 1\n"
"v_mov_b32_dpp v16, v17 quad_perm:[0,1,2,3] row_mask:0x2 bank_mask:0xf\n"
"s_lshl_b32 s70, s50, 7\n"
"s_add_u32 s70, s70, 3584\n"
"s_mov_b32 exec_lo, 0\n"
"v_lshl_add_u32 v16, v0, 4, s70\n"
"s_mov_b32 exec_lo, -1\n"
"v_mov_b32 v11, s66\n"
"s_waitcnt lgkmcnt(2)\n"
"v_mfma_f32_16x16x32_bf16 v[28:31], v[18:21], v[68:71], 0\n"
"v_mfma_f32_16x16x32_bf16 v[32:35], v[18:21], v[78:81], 0\n"
"v_mfma_f32_16x16x32_bf16 v[36:39], v[18:21], v[88:91], 0\n"
"v_mfma_f32_16x16x32_bf16 v[40:43], v[18:21], v[18:21], 0\n"
"s_waitcnt lgkmcnt(1)\n"
"v_mfma_f32_16x16x32_bf16 v[28:31], v[22:25], v[72:75], v[28:31]\n"
"v_mfma_f32_16x16x32_bf16 v[32:35], v[22:25], v[82:85], v[32:35]\n"
"v_mfma_f32_16x16x32_bf16 v[36:39], v[22:25], v[92:95], v[36:39]\n"
"v_mfma_f32_16x16x32_bf16 v[40:43], v[22:25], v[22:25], v[40:43]\n"
"s_waitcnt lgkmcnt(0)\n"
"v_mfma_f32_16x16x16_bf16 v[28:31], v[26:27], v[76:77], v[28:31]\n"
"v_mfma_f32_16x16x16_bf16 v[32:35], v[26:27], v[86:87], v[32:35]\n"
"v_mfma_f32_16x16x16_bf16 v[36:39], v[26:27], v[96:97], v[36:39]\n"
"v_mfma_f32_16x16x16_bf16 v[40:43], v[26:27], v[26:27], v[40:43]\n"
"v_mov_b32_dpp v28, v29 quad_perm:[0,1,2,3] row_mask:0x2 bank_mask:0xf\n"
"v_mov_b32_dpp v32, v33 quad_perm:[0,1,2,3] row_mask:0x2 bank_mask:0xf\n"
"s_nop 1\n"
"v_mov_b32_dpp v36, v37 quad_perm:[0,1,2,3] row_mask:0x2 bank_mask:0xf\n"
"v_mov_b32_dpp v29, v28 quad_perm:[1,1,3,3] row_mask:0xf bank_mask:0xf\n"
"v_mov_b32_dpp v33, v32 quad_perm:[1,1,3,3] row_mask:0xf bank_mask:0xf\n"
"v_mov_b32_dpp v37, v36 quad_perm:[1,1,3,3] row_mask:0xf bank_mask:0xf\n"
"v_cvt_pk_bf16_f32 v8, v28, v29\n"
"v_cvt_pk_bf16_f32 v9, v32, v33\n"
"v_cvt_pk_bf16_f32 v10, v36, v37\n"
"v_mov_b32_dpp v8, v40 quad_perm:[0,1,2,3] row_mask:0x4 bank_mask:0xf\n"
"s_mov_b64 exec, s[78:79]\n"
"buffer_store_dwordx4 v[8:11], v16, s[40:43], s56 offen offset:0 sc1\n"
"s_mov_b64 exec, -1\n"
"v_and_b32 v6, 1, v0\n"
"v_mul_u32_u24 v6, 0xaaaa, v6\n"
"v_add_u32 v6, 0x4444, v6\n"
"s_cmp_eq_u32 s48, 1\n"
"s_cbranch_scc0 .Lr_90_%=\n"
"v_and_b32 v38, 15, v0\n"
"v_lshlrev_b32 v38, 1, v38\n"
"s_mov_b32 s70, 0x44ee44\n"
"v_bfe_u32 v1, s70, v38, 2\n"
"s_mov_b32 s70, 0xf5a0a0\n"
"v_bfe_u32 v38, s70, v38, 2\n"
"v_mul_u32_u24 v1, 5, v1\n"
"v_lshlrev_b32 v2, 2, v38\n"
"v_lshlrev_b32 v1, v2, v1\n"
"v_lshlrev_b32 v38, v38, 1\n"
"v_bfe_i32 v2, v38, 0, 1\n"
"v_and_b32 v2, 0x3f80, v2\n"
"v_bfe_i32 v3, v38, 1, 1\n"
"v_and_b32 v3, 0x3f80, v3\n"
"v_bfe_i32 v4, v38, 2, 1\n"
"v_and_b32 v4, 0x3f80, v4\n"
"v_bfe_i32 v5, v38, 3, 1\n"
"v_and_b32 v5, 0x3f80, v5\n"
"v_and_b32 v7, 15, v0\n"
"v_bfe_u32 v38, v0, 4, 1\n"
"v_lshl_add_u32 v7, v7, 1, v38\n"
"v_and_b32 v38, 32, v0\n"
"v_add_lshl_u32 v7, v7, v38, 4\n"
"v_mov_b64 v[28:29], 0\n"
"v_mov_b64 v[30:31], 0\n"
"v_add_u32 v7, 4096, v7\n"
"v_lshlrev_b32 v39, 4, v0\n"
".Lr_90_%=:\n"
"s_barrier\n"
"s_cmp_lg_u32 s48, 0\n"
"s_cbranch_scc0 .Lr_91_%=\n"
"v_lshl_add_u32 v40, s48, 5, v0\n"
"v_subrev_u32 v40, 32, v40\n"
"v_lshrrev_b32 v40, 6, v40\n"
"v_xor_b32 v40, v0, v40\n"
"v_lshlrev_b32 v40, 4, v40\n"
"s_lshl_b32 s70, s48, 10\n"
"s_add_u32 s70, s70, 60416\n"
"s_add_u32 m0, s70, 65536\n"
"v_xor_b32 v41, 128, v40\n"
"buffer_load_dwordx4 v41, s[28:31], s70 offen offset:0 lds\n"
"s_add_u32 s70, s70, 3072\n"
"s_add_u32 m0, s70, 65536\n"
"v_xor_b32 v41, 160, v40\n"
"buffer_load_dwordx4 v41, s[28:31], s70 offen offset:0 lds\n"
"s_add_u32 s70, s70, 3072\n"
"s_add_u32 m0, s70, 65536\n"
"v_xor_b32 v41, 192, v40\n"
"buffer_load_dwordx4 v41, s[28:31], s70 offen offset:0 lds\n"
"s_add_u32 s70, s70, 3072\n"
"s_add_u32 m0, s70, 65536\n"
"v_xor_b32 v41, 224, v40\n"
"buffer_load_dwordx4 v41, s[28:31], s70 offen offset:0 lds\n"
"s_add_u32 s70, s70, 3072\n"
"s_add_u32 m0, s70, 65536\n"
"v_xor_b32 v41, 0, v40\n"
"buffer_load_dwordx4 v41, s[28:31], s70 offen offset:0 lds\n"
"s_add_u32 s70, s70, 3072\n"
"s_add_u32 m0, s70, 65536\n"
"v_xor_b32 v41, 32, v40\n"
"buffer_load_dwordx4 v41, s[28:31], s70 offen offset:0 lds\n"
"s_add_u32 s70, s70, 3072\n"
"s_add_u32 m0, s70, 65536\n"
"v_xor_b32 v41, 64, v40\n"
"buffer_load_dwordx4 v41, s[28:31], s70 offen offset:0 lds\n"
"s_add_u32 s70, s70, 3072\n"
"s_add_u32 m0, s70, 65536\n"
"v_xor_b32 v41, 96, v40\n"
"buffer_load_dwordx4 v41, s[28:31], s70 offen offset:0 lds\n"
"s_add_u32 s70, s70, 3072\n"
"s_add_u32 m0, s70, 65536\n"
"v_xor_b32 v41, 128, v40\n"
"buffer_load_dwordx4 v41, s[28:31], s70 offen offset:0 lds\n"
"s_add_u32 s70, s70, 3072\n"
"s_add_u32 m0, s70, 65536\n"
"v_xor_b32 v41, 160, v40\n"
"buffer_load_dwordx4 v41, s[28:31], s70 offen offset:0 lds\n"
"s_add_u32 s70, s70, 3072\n"
"s_add_u32 m0, s70, 65536\n"
"v_xor_b32 v41, 192, v40\n"
"buffer_load_dwordx4 v41, s[28:31], s70 offen offset:0 lds\n"
"s_add_u32 s70, s70, 3072\n"
"s_add_u32 m0, s70, 65536\n"
"v_xor_b32 v41, 224, v40\n"
"buffer_load_dwordx4 v41, s[28:31], s70 offen offset:0 lds\n"
".Lr_91_%=:\n"
"s_cmp_eq_u32 s48, 0\n"
"s_cbranch_scc0 .Lr_92_%=\n"
"v_and_b32 v38, 15, v0\n"
"v_lshlrev_b32 v38, 1, v38\n"
"s_mov_b32 s70, 0x44ee44\n"
"v_bfe_u32 v1, s70, v38, 2\n"
"s_mov_b32 s70, 0xf5a0a0\n"
"v_bfe_u32 v38, s70, v38, 2\n"
"v_mul_u32_u24 v1, 5, v1\n"
"v_lshlrev_b32 v2, 2, v38\n"
"v_lshlrev_b32 v1, v2, v1\n"
"v_lshlrev_b32 v38, v38, 1\n"
"v_bfe_i32 v2, v38, 0, 1\n"
"v_and_b32 v2, 0x3f80, v2\n"
"v_bfe_i32 v3, v38, 1, 1\n"
"v_and_b32 v3, 0x3f80, v3\n"
"v_bfe_i32 v4, v38, 2, 1\n"
"v_and_b32 v4, 0x3f80, v4\n"
"v_bfe_i32 v5, v38, 3, 1\n"
"v_and_b32 v5, 0x3f80, v5\n"
"v_and_b32 v7, 15, v0\n"
"v_bfe_u32 v38, v0, 4, 1\n"
"v_lshl_add_u32 v7, v7, 1, v38\n"
"v_and_b32 v38, 32, v0\n"
"v_add_lshl_u32 v7, v7, v38, 4\n"
"v_mov_b64 v[28:29], 0\n"
"v_mov_b64 v[30:31], 0\n"
"v_bfe_u32 v38, v0, 2, 2\n"
"v_lshrrev_b32 v36, 4, v0\n"
"v_mad_u32_u24 v38, v38, 3, v36\n"
"v_and_b32 v36, 3, v0\n"
"v_lshl_add_u32 v36, v38, 3, v36\n"
"v_lshlrev_b32 v36, 2, v36\n"
"v_lshrrev_b32 v42, 4, v0\n"
"v_and_b32 v38, 1, v0\n"
"v_lshl_add_u32 v42, v42, 1, v38\n"
"v_lshlrev_b32 v42, 4, v42\n"
"v_and_b32 v37, 14, v0\n"
"v_and_b32 v39, 1, v0\n"
"v_lshl_add_u32 v37, v37, 2, v39\n"
"v_lshlrev_b32 v37, 4, v37\n"
"v_add_u32 v37, 4096, v37\n"
"v_lshlrev_b32 v39, 4, v0\n"
".Lr_92_%=:\n"
"s_cmp_lg_u32 s48, 0\n"
"s_cbranch_scc0 .Lr_93_%=\n"
"s_waitcnt vmcnt(6)\n"
".Lr_93_%=:\n"
"s_barrier\n"
"s_cmp_eq_u32 s48, 0\n"
"s_cbranch_scc0 .Lr_94_%=\n"
"s_sleep 2\n"
"buffer_load_dwordx4 v[8:11], v7, s[40:43], s56 offen offset:0 sc1\n"
"buffer_load_dwordx4 v[12:15], v7, s[40:43], s56 offen offset:1024 sc1\n"
"buffer_load_dwordx4 v[16:19], v7, s[40:43], s56 offen offset:2048 sc1\n"
"buffer_load_dwordx4 v[20:23], v7, s[40:43], s56 offen offset:3072 sc1\n"
"buffer_load_dwordx4 v[24:27], v37, s[40:43], s56 offen offset:0 sc1\n"
".Lr_94_%=:\n"
"s_cmp_eq_u32 s48, 1\n"
"s_cbranch_scc0 .Lr_95_%=\n"
"s_sleep 2\n"
"buffer_load_dwordx4 v[8:11], v7, s[40:43], s57 offen offset:0 sc1\n"
"buffer_load_dwordx4 v[12:15], v7, s[40:43], s57 offen offset:1024 sc1\n"
"buffer_load_dwordx4 v[16:19], v7, s[40:43], s57 offen offset:2048 sc1\n"
"buffer_load_dwordx4 v[20:23], v7, s[40:43], s57 offen offset:3072 sc1\n"
".Lr_95_%=:\n"
"s_barrier\n"
"s_cmp_lg_u32 s48, 0\n"
"s_cbranch_scc0 .Lr_96_%=\n"
"s_sleep 3\n"
"s_mov_b32 m0, 768\n"
"s_mul_i32 s70, s48, 32\n"
"s_add_u32 s70, s70, 4294967264\n"
"v_add_u32 v41, s70, v0\n"
"v_lshlrev_b32 v41, 7, v41\n"
"s_mul_i32 s70, s52, 24576\n"
"s_mov_b32 exec_lo, 0xffffffff\n"
"s_mov_b32 exec_hi, 0x0\n"
"buffer_load_dword v41, s[28:31], s70 offen lds\n"
"s_mov_b64 exec, -1\n"
".Lr_96_%=:\n"
"s_cmp_lg_u32 s48, 0\n"
"s_cbranch_scc0 .Lr_97_%=\n"
"s_cmp_eq_u32 s48, 1\n"
"s_cbranch_scc0 .Lr_98_%=\n"
".Lgb_99_%=:\n"
"s_waitcnt vmcnt(3)\n"
"v_smfmac_f32_16x16x64_bf16 v[28:31], v[2:5], v[8:15], v1\n"
"s_waitcnt vmcnt(1)\n"
"v_smfmac_f32_16x16x64_bf16 v[28:31], v[2:5], v[16:23], v1\n"
"v_bitop3_b32 v38, v11, v15, s66 bitop3:0x7e\n"
"v_bitop3_b32 v38, v19, v38, s66 bitop3:0xde\n"
"v_bitop3_b32 v38, v23, v38, s66 bitop3:0xde\n"
"v_cmp_ne_u32 vcc, 0, v38\n"
"s_cbranch_vccnz .Lgr_100_%=\n"
".Lgo_101_%=:\n"
"s_nop 2\n"
"v_mul_f32 v28, 0xbacccccd, v28\n"
"v_mul_f32 v29, 0xbacccccd, v29\n"
"v_mul_f32 v30, 0xbacccccd, v30\n"
"v_mul_f32 v31, 0xbacccccd, v31\n"
"s_mov_b32 exec_hi, 0xffff\n"
"ds_write_b128 v39, v[28:31] offset:1792\n"
"s_mov_b32 exec_hi, -1\n"
"s_waitcnt lgkmcnt(0)\n"
".Lr_98_%=:\n"
"v_lshrrev_b32 v42, 4, v0\n"
"v_and_b32 v38, 1, v0\n"
"v_lshl_add_u32 v42, v42, 1, v38\n"
"v_lshlrev_b32 v42, 4, v42\n"
"s_mul_i32 s70, s48, 12288\n"
"s_add_u32 s70, s70, -2048\n"
"v_and_b32 v7, 15, v0\n"
"v_lshrrev_b32 v40, 1, v7\n"
"v_lshrrev_b32 v41, 4, v0\n"
"v_xor_b32 v40, v40, v41\n"
"v_mul_u32_u24 v7, 24, v7\n"
"v_add_lshl_u32 v40, v40, v7, 4\n"
"v_add_u32 v40, s70, v40\n"
"v_xor_b32 v41, 64, v40\n"
".Lr_97_%=:\n"
"s_cmp_eq_u32 s48, 0\n"
"s_cbranch_scc0 .Lr_102_%=\n"
".Lgb_103_%=:\n"
"s_waitcnt vmcnt(3)\n"
"v_smfmac_f32_16x16x64_bf16 v[28:31], v[2:5], v[8:15], v1\n"
"s_waitcnt vmcnt(1)\n"
"v_smfmac_f32_16x16x64_bf16 v[28:31], v[2:5], v[16:23], v1\n"
"s_waitcnt vmcnt(0)\n"
"v_add_f32_dpp v24, v24, v24 row_ror:2 row_mask:0xf bank_mask:0xf\n"
"v_bitop3_b32 v38, v11, v15, s66 bitop3:0x7e\n"
"v_bitop3_b32 v38, v19, v38, s66 bitop3:0xde\n"
"v_add_f32_dpp v24, v24, v24 row_ror:4 row_mask:0xf bank_mask:0xf\n"
"v_bitop3_b32 v38, v23, v38, s66 bitop3:0xde\n"
"v_bitop3_b32 v38, v27, v38, s66 bitop3:0xde\n"
"v_add_f32_dpp v24, v24, v24 row_ror:8 row_mask:0xf bank_mask:0xf\n"
"v_cmp_ne_u32 vcc, 0, v38\n"
"s_cbranch_vccnz .Lgr_104_%=\n"
".Lgo_105_%=:\n"
".Lr_102_%=:\n"
"s_barrier\n"
"s_cmp_eq_u32 s48, 0\n"
"s_cbranch_scc0 .Lr_106_%=\n"
"ds_read_b128 v[32:35], v39 offset:1792\n"
"s_waitcnt lgkmcnt(0)\n"
"v_fmac_f32_dpp v28, v24, v32 quad_perm:[1,1,3,3] row_mask:0xf bank_mask:0xf\n"
"v_fmac_f32_dpp v29, v24, v33 quad_perm:[1,1,3,3] row_mask:0xf bank_mask:0xf\n"
"v_fmac_f32_dpp v30, v24, v34 quad_perm:[1,1,3,3] row_mask:0xf bank_mask:0xf\n"
"v_fmac_f32_dpp v31, v24, v35 quad_perm:[1,1,3,3] row_mask:0xf bank_mask:0xf\n"
"v_cvt_pk_bf16_f32 v40, v28, v29\n"
"v_cvt_pk_bf16_f32 v41, v30, v31\n"
"s_mov_b32 exec_hi, 0xffff\n"
"ds_write2_b32 v36, v40, v41 offset0:96 offset1:100\n"
"s_mov_b64 exec, 3\n"
"ds_write_b32 v39, v24 offset:320\n"
"s_mov_b64 exec, -1\n"
"s_waitcnt lgkmcnt(0)\n"
".Lr_106_%=:\n"
"s_cmp_lg_u32 s48, 0\n"
"s_cbranch_scc0 .Lr_107_%=\n"
"ds_read_b128 v[120:123], v40 offset:0\n"
"ds_read_b128 v[124:127], v41 offset:0\n"
"ds_read_b128 v[20:23], v40 offset:128\n"
"ds_read_b128 v[24:27], v41 offset:128\n"
"ds_read_b128 v[28:31], v40 offset:256\n"
"ds_read_b128 v[32:35], v41 offset:256\n"
".Lr_107_%=:\n"
"s_barrier\n"
"s_cmp_lg_u32 s48, 0\n"
"s_cbranch_scc0 .Lr_108_%=\n"
"s_mov_b32 m0, 768\n"
"s_mul_i32 s70, s48, 16\n"
"s_add_u32 s70, s70, 4294967280\n"
"v_add_u32 v2, s70, v0\n"
"v_mul_u32_u24 v3, 683, v2\n"
"v_lshrrev_b32 v3, 11, v3\n"
"v_mul_u32_u24 v3, 11904, v3\n"
"v_lshl_add_u32 v2, v2, 7, v3\n"
"s_mov_b32 exec_lo, 0xffff\n"
"s_mov_b32 exec_hi, 0x0\n"
"buffer_load_dword v2, s[32:35], 0 offen lds\n"
"s_mov_b64 exec, -1\n"
"s_mov_b32 m0, 768\n"
"s_mul_i32 s70, s48, 16\n"
"s_add_u32 s70, s70, 128\n"
"v_add_u32 v2, s70, v0\n"
"v_mul_u32_u24 v3, 683, v2\n"
"v_lshrrev_b32 v3, 11, v3\n"
"v_mul_u32_u24 v3, 11904, v3\n"
"v_lshl_add_u32 v2, v2, 7, v3\n"
"s_mov_b32 exec_lo, 0xffff\n"
"s_mov_b32 exec_hi, 0x0\n"
"buffer_load_dword v2, s[32:35], 0 offen lds\n"
"s_mov_b64 exec, -1\n"
".Lr_108_%=:\n"
"s_cmp_lg_u32 s48, 0\n"
"s_cbranch_scc0 .Lr_109_%=\n"
"ds_read_b128 v[8:11], v42 offset:384\n"
"ds_read_b128 v[12:15], v42 offset:512\n"
"ds_read_b128 v[16:19], v42 offset:640\n"
"v_mov_b32 v7, 0\n"
"ds_read2_b32 v[42:43], v7 offset0:80 offset1:84\n"
"v_mov_b64 v[2:3], 0\n"
"v_mov_b64 v[36:37], 0\n"
"v_lshlrev_b32 v1, 2, v0\n"
"s_waitcnt lgkmcnt(3)\n"
"v_smfmac_f32_16x16x64_bf16 v[2:5], v[8:11], v[120:127], v6\n"
"ds_read_b128 v[120:123], v40 offset:6144\n"
"ds_read_b128 v[124:127], v41 offset:6144\n"
"s_waitcnt lgkmcnt(4)\n"
"v_smfmac_f32_16x16x64_bf16 v[2:5], v[12:15], v[20:27], v6\n"
"ds_read_b128 v[20:23], v40 offset:6272\n"
"ds_read_b128 v[24:27], v41 offset:6272\n"
"s_waitcnt lgkmcnt(4)\n"
"v_smfmac_f32_16x16x64_bf16 v[2:5], v[16:19], v[28:35], v6\n"
"ds_read_b128 v[28:31], v40 offset:6400\n"
"ds_read_b128 v[32:35], v41 offset:6400\n"
"v_mul_f32 v43, 0x3acccccd, v43\n"
"v_mul_f32 v42, 0x3acccccd, v42\n"
"v_fma_f32 v42, -v43, v43, v42\n"
"s_waitcnt lgkmcnt(4)\n"
"v_smfmac_f32_16x16x64_bf16 v[36:39], v[8:11], v[120:127], v6\n"
"v_max_f32 v42, 0, v42\n"
"v_add_f32 v42, 0x3727c5ac, v42\n"
"v_rsq_f32 v42, v42\n"
"s_waitcnt lgkmcnt(2)\n"
"v_smfmac_f32_16x16x64_bf16 v[36:39], v[12:15], v[20:27], v6\n"
"v_mul_f32 v42, 0x3dd53b95, v42\n"
"s_waitcnt lgkmcnt(0)\n"
"v_smfmac_f32_16x16x64_bf16 v[36:39], v[16:19], v[28:35], v6\n"
"s_mov_b64 exec, 0xffff\n"
"v_add_f32 v2, v2, v3\n"
"s_nop 5\n"
"v_add_f32 v36, v36, v37\n"
"v_mul_f32 v2, v2, v42\n"
"v_mul_f32 v36, v36, v42\n"
"v_cvt_pk_bf16_f32 v2, v2, v36\n"
"buffer_store_dword v2, v1, s[40:43], s61 offen offset:0 sc1\n"
"s_mov_b64 exec, -1\n"
".Lr_109_%=:\n"
"s_cmp_eq_u32 s48, 0\n"
"s_cbranch_scc0 .Lr_110_%=\n"
".Lr_110_%=:\n"
"s_cmp_lg_u32 s48, 0\n"
"s_cbranch_scc0 .Lr_111_%=\n"
"v_lshl_add_u32 v28, s48, 5, v0\n"
"v_subrev_u32 v28, 32, v28\n"
"v_lshrrev_b32 v28, 6, v28\n"
"v_xor_b32 v28, v0, v28\n"
"v_lshlrev_b32 v28, 4, v28\n"
"s_lshl_b32 s70, s48, 10\n"
"s_add_u32 s70, s70, -1024\n"
"s_add_u32 m0, s70, 65536\n"
"v_xor_b32 v29, 0, v28\n"
"buffer_load_dwordx4 v29, s[28:31], s70 offen offset:0 lds\n"
"s_add_u32 s70, s70, 3072\n"
"s_add_u32 m0, s70, 65536\n"
"v_xor_b32 v29, 32, v28\n"
"buffer_load_dwordx4 v29, s[28:31], s70 offen offset:0 lds\n"
"s_add_u32 s70, s70, 3072\n"
"s_add_u32 m0, s70, 65536\n"
"v_xor_b32 v29, 64, v28\n"
"buffer_load_dwordx4 v29, s[28:31], s70 offen offset:0 lds\n"
"s_add_u32 s70, s70, 3072\n"
"s_add_u32 m0, s70, 65536\n"
"v_xor_b32 v29, 96, v28\n"
"buffer_load_dwordx4 v29, s[28:31], s70 offen offset:0 lds\n"
"s_add_u32 s70, s70, 3072\n"
"s_add_u32 m0, s70, 65536\n"
"v_xor_b32 v29, 128, v28\n"
"buffer_load_dwordx4 v29, s[28:31], s70 offen offset:0 lds\n"
"s_add_u32 s70, s70, 3072\n"
"s_add_u32 m0, s70, 65536\n"
"v_xor_b32 v29, 160, v28\n"
"buffer_load_dwordx4 v29, s[28:31], s70 offen offset:0 lds\n"
"s_add_u32 s70, s70, 3072\n"
"s_add_u32 m0, s70, 65536\n"
"v_xor_b32 v29, 192, v28\n"
"buffer_load_dwordx4 v29, s[28:31], s70 offen offset:0 lds\n"
"s_add_u32 s70, s70, 3072\n"
"s_add_u32 m0, s70, 65536\n"
"v_xor_b32 v29, 224, v28\n"
"buffer_load_dwordx4 v29, s[28:31], s70 offen offset:0 lds\n"
"s_add_u32 s70, s70, 3072\n"
"s_add_u32 m0, s70, 65536\n"
"v_xor_b32 v29, 0, v28\n"
"buffer_load_dwordx4 v29, s[28:31], s70 offen offset:0 lds\n"
"s_add_u32 s70, s70, 3072\n"
"s_add_u32 m0, s70, 65536\n"
"v_xor_b32 v29, 32, v28\n"
"buffer_load_dwordx4 v29, s[28:31], s70 offen offset:0 lds\n"
"s_add_u32 s70, s70, 3072\n"
"s_add_u32 m0, s70, 65536\n"
"v_xor_b32 v29, 64, v28\n"
"buffer_load_dwordx4 v29, s[28:31], s70 offen offset:0 lds\n"
"s_add_u32 s70, s70, 3072\n"
"s_add_u32 m0, s70, 65536\n"
"v_xor_b32 v29, 96, v28\n"
"buffer_load_dwordx4 v29, s[28:31], s70 offen offset:0 lds\n"
"s_add_u32 s70, s70, 3072\n"
"s_add_u32 m0, s70, 65536\n"
"v_xor_b32 v29, 128, v28\n"
"buffer_load_dwordx4 v29, s[28:31], s70 offen offset:0 lds\n"
"s_add_u32 s70, s70, 3072\n"
"s_add_u32 m0, s70, 65536\n"
"v_xor_b32 v29, 160, v28\n"
"buffer_load_dwordx4 v29, s[28:31], s70 offen offset:0 lds\n"
"s_add_u32 s70, s70, 3072\n"
"s_add_u32 m0, s70, 65536\n"
"v_xor_b32 v29, 192, v28\n"
"buffer_load_dwordx4 v29, s[28:31], s70 offen offset:0 lds\n"
"s_add_u32 s70, s70, 3072\n"
"s_add_u32 m0, s70, 65536\n"
"v_xor_b32 v29, 224, v28\n"
"buffer_load_dwordx4 v29, s[28:31], s70 offen offset:0 lds\n"
".Lr_111_%=:\n"
"v_bfe_u32 v5, v0, 1, 3\n"
"v_mul_u32_u24 v5, 768, v5\n"
"v_and_b32 v7, 48, v0\n"
"v_lshl_add_u32 v7, v7, 1, v5\n"
"v_and_b32 v5, 1, v0\n"
"v_lshl_add_u32 v7, v5, 4, v7\n"
"s_mul_i32 s70, s48, 2304\n"
"s_add_u32 s70, s70, 56320\n"
"v_lshrrev_b32 v5, 4, v0\n"
"v_mul_u32_u24 v5, 144, v5\n"
"v_and_or_b32 v5, v0, 15, v5\n"
"v_lshl_add_u32 v5, v5, 2, s70\n"
"v_mov_b64 v[20:21], 0\n"
"v_mov_b64 v[22:23], 0\n"
"v_and_b32 v3, 15, v0\n"
"v_lshrrev_b32 v4, 4, v0\n"
"v_xor_b32 v4, v4, v3\n"
"v_mul_u32_u24 v3, 1536, v3\n"
"v_lshl_add_u32 v3, v4, 4, v3\n"
"s_and_b32 s70, s48, 1\n"
"s_lshl_b32 s70, s70, 7\n"
"s_mul_i32 s71, s48, 384\n"
"v_xor_b32 v1, s70, v3\n"
"s_andn2_b32 s72, s71, 255\n"
"s_add_u32 s72, s72, 65536\n"
"v_xor_b32 v3, 128, v1\n"
"v_add_u32 v1, s72, v1\n"
"s_add_u32 s71, s71, 128\n"
"s_andn2_b32 s72, s71, 255\n"
"s_add_u32 s72, s72, 65536\n"
"v_add_u32 v3, s72, v3\n"
"v_xor_b32 v2, 64, v1\n"
"v_xor_b32 v4, 64, v3\n"
"s_cmp_lg_u32 s48, 0\n"
"s_cbranch_scc0 .Lr_112_%=\n"
"s_waitcnt vmcnt(4)\n"
".Lr_112_%=:\n"
"s_barrier\n"
"buffer_load_dwordx4 v[8:11], v7, s[40:43], s60 offen offset:0 sc1\n"
"buffer_load_dwordx4 v[12:15], v7, s[40:43], s60 offen offset:128 sc1\n"
"buffer_load_dwordx4 v[16:19], v7, s[40:43], s60 offen offset:256 sc1\n"
"s_cmp_lg_u32 s48, 0\n"
"s_cbranch_scc0 .Lr_113_%=\n"
"s_waitcnt vmcnt(3)\n"
".Lr_113_%=:\n"
"s_barrier\n"
"ds_read_b128 v[120:123], v1 offset:0\n"
"ds_read_b128 v[124:127], v2 offset:0\n"
"ds_read_b128 v[28:31], v3 offset:0\n"
"ds_read_b128 v[32:35], v4 offset:0\n"
"ds_read_b128 v[36:39], v1 offset:256\n"
"ds_read_b128 v[40:43], v2 offset:256\n"
".Lgb_114_%=:\n"
"s_waitcnt vmcnt(0)\n"
"v_max3_u32 v24, v8, v9, v10\n"
"v_max3_u32 v25, v11, v12, v13\n"
"v_max3_u32 v24, v24, v14, v15\n"
"v_max3_u32 v25, v25, v16, v17\n"
"v_max3_u32 v24, v24, v18, v19\n"
"v_max_u32 v24, v24, v25\n"
"v_cmp_eq_u32 vcc, -1, v24\n"
"s_cbranch_vccnz .Lgr_115_%=\n"
".Lgo_116_%=:\n"
"s_waitcnt lgkmcnt(4)\n"
"v_smfmac_f32_16x16x64_bf16 v[20:23], v[8:11], v[120:127], v6\n"
"v_mov_b64 v[24:25], 0\n"
"v_mov_b64 v[26:27], 0\n"
"ds_read_b128 v[120:123], v1 offset:24576\n"
"ds_read_b128 v[124:127], v2 offset:24576\n"
"s_waitcnt lgkmcnt(4)\n"
"v_smfmac_f32_16x16x64_bf16 v[20:23], v[12:15], v[28:35], v6\n"
"ds_read_b128 v[28:31], v3 offset:24576\n"
"ds_read_b128 v[32:35], v4 offset:24576\n"
"s_waitcnt lgkmcnt(4)\n"
"v_smfmac_f32_16x16x64_bf16 v[20:23], v[16:19], v[36:43], v6\n"
"ds_read_b128 v[36:39], v1 offset:24832\n"
"ds_read_b128 v[40:43], v2 offset:24832\n"
"s_waitcnt lgkmcnt(4)\n"
"v_smfmac_f32_16x16x64_bf16 v[24:27], v[8:11], v[120:127], v6\n"
"v_add_u32 v1, 49152, v1\n"
"v_add_u32 v2, 49152, v2\n"
"v_add_u32 v3, 49152, v3\n"
"v_add_u32 v4, 49152, v4\n"
"ds_read_b128 v[120:123], v1 offset:0\n"
"ds_read_b128 v[124:127], v2 offset:0\n"
"s_waitcnt lgkmcnt(4)\n"
"v_smfmac_f32_16x16x64_bf16 v[24:27], v[12:15], v[28:35], v6\n"
"ds_read_b128 v[28:31], v3 offset:0\n"
"ds_read_b128 v[32:35], v4 offset:0\n"
"v_add_f32 v20, v20, v21\n"
"v_add_f32 v22, v22, v23\n"
"ds_write2_b32 v5, v20, v22 offset0:0 offset1:72\n"
"v_mov_b64 v[20:21], 0\n"
"v_mov_b64 v[22:23], 0\n"
"s_waitcnt lgkmcnt(5)\n"
"v_smfmac_f32_16x16x64_bf16 v[24:27], v[16:19], v[36:43], v6\n"
"ds_read_b128 v[36:39], v1 offset:256\n"
"ds_read_b128 v[40:43], v2 offset:256\n"
"s_waitcnt lgkmcnt(5)\n"
"v_smfmac_f32_16x16x64_bf16 v[20:23], v[8:11], v[120:127], v6\n"
"ds_read_b128 v[120:123], v1 offset:24576\n"
"ds_read_b128 v[124:127], v2 offset:24576\n"
"s_waitcnt lgkmcnt(5)\n"
"v_smfmac_f32_16x16x64_bf16 v[20:23], v[12:15], v[28:35], v6\n"
"ds_read_b128 v[28:31], v3 offset:24576\n"
"ds_read_b128 v[32:35], v4 offset:24576\n"
"v_add_f32 v24, v24, v25\n"
"v_add_f32 v26, v26, v27\n"
"ds_write2_b32 v5, v24, v26 offset0:16 offset1:88\n"
"v_mov_b64 v[24:25], 0\n"
"v_mov_b64 v[26:27], 0\n"
"s_waitcnt lgkmcnt(5)\n"
"v_smfmac_f32_16x16x64_bf16 v[20:23], v[16:19], v[36:43], v6\n"
"ds_read_b128 v[36:39], v1 offset:24832\n"
"ds_read_b128 v[40:43], v2 offset:24832\n"
"s_waitcnt lgkmcnt(5)\n"
"v_smfmac_f32_16x16x64_bf16 v[24:27], v[8:11], v[120:127], v6\n"
"s_waitcnt lgkmcnt(3)\n"
"v_smfmac_f32_16x16x64_bf16 v[24:27], v[12:15], v[28:35], v6\n"
"v_add_f32 v20, v20, v21\n"
"v_add_f32 v22, v22, v23\n"
"ds_write2_b32 v5, v20, v22 offset0:32 offset1:104\n"
"s_waitcnt lgkmcnt(1)\n"
"v_smfmac_f32_16x16x64_bf16 v[24:27], v[16:19], v[36:43], v6\n"
"v_bfe_u32 v7, v0, 1, 3\n"
"v_mul_u32_u24 v7, 18, v7\n"
"v_and_or_b32 v7, v0, 1, v7\n"
"v_and_b32 v1, 48, v0\n"
"v_lshlrev_b32 v1, 2, v1\n"
"v_lshl_add_u32 v7, v7, 4, v1\n"
"s_nop 1\n"
"v_add_f32 v24, v24, v25\n"
"v_add_f32 v26, v26, v27\n"
"ds_write2_b32 v5, v24, v26 offset0:48 offset1:120\n"
"s_cmp_lg_u32 s48, 0\n"
"s_cbranch_scc0 .Lr_117_%=\n"
"s_mov_b32 m0, 768\n"
"s_mul_i32 s70, s48, 32\n"
"s_add_u32 s70, s70, 16\n"
"v_add_u32 v29, s70, v0\n"
"v_mul_u32_u24 v30, 683, v29\n"
"v_lshrrev_b32 v30, 11, v30\n"
"v_mul_u32_u24 v30, 11904, v30\n"
"v_lshl_add_u32 v29, v29, 7, v30\n"
"s_mov_b32 exec_lo, 0xffffffff\n"
"s_mov_b32 exec_hi, 0x0\n"
"buffer_load_dword v29, s[32:35], 0 offen lds\n"
"s_mov_b64 exec, -1\n"
"s_mov_b32 m0, 768\n"
"s_mul_i32 s70, s48, 16\n"
"s_add_u32 s70, s70, 4294967280\n"
"v_add_u32 v29, s70, v0\n"
"v_lshlrev_b32 v29, 7, v29\n"
"s_mul_i32 s70, s50, 30560\n"
"s_mov_b32 exec_lo, 0xffff\n"
"s_mov_b32 exec_hi, 0x0\n"
"buffer_load_dword v29, s[36:39], s70 offen lds\n"
"s_mov_b64 exec, -1\n"
".Lr_117_%=:\n"
"s_waitcnt lgkmcnt(0)\n"
"s_barrier\n"
"ds_read_b128 v[8:11], v7 offset:56320\n"
"ds_read_b128 v[12:15], v7 offset:56352\n"
"ds_read_b128 v[16:19], v7 offset:58624\n"
"ds_read_b128 v[20:23], v7 offset:58656\n"
"ds_read_b128 v[24:27], v7 offset:60928\n"
"ds_read_b128 v[28:31], v7 offset:60960\n"
"ds_read_b128 v[32:35], v7 offset:63232\n"
"ds_read_b128 v[36:39], v7 offset:63264\n"
"v_lshrrev_b32 v4, 1, v0\n"
"v_and_b32 v5, 15, v4\n"
"v_and_b32 v4, 30, v4\n"
"v_mul_u32_u24 v4, 1536, v4\n"
"v_lshl_add_u32 v4, v5, 4, v4\n"
"v_and_b32 v5, 1, v0\n"
"v_lshl_add_u32 v4, v5, 3, v4\n"
"s_and_b32 s70, s48, 1\n"
"s_lshl_b32 s70, s70, 7\n"
"s_mul_i32 s71, s48, 384\n"
"v_xor_b32 v4, s70, v4\n"
"s_andn2_b32 s72, s71, 255\n"
"s_add_u32 s72, s72, 65536\n"
"v_xor_b32 v5, 128, v4\n"
"v_add_u32 v4, s72, v4\n"
"s_add_u32 s71, s71, 128\n"
"s_andn2_b32 s72, s71, 255\n"
"s_add_u32 s72, s72, 65536\n"
"v_add_u32 v5, s72, v5\n"
"s_add_u32 s66, s54, 4\n"
"v_xor_b32 v7, 0, v4\n"
"v_xor_b32 v1, 16, v4\n"
"ds_read_b64_tr_b16 v[120:121], v7 offset:0\n"
"ds_read_b64_tr_b16 v[122:123], v1 offset:1536\n"
"ds_read_b64_tr_b16 v[124:125], v7 offset:49152\n"
"ds_read_b64_tr_b16 v[126:127], v1 offset:50688\n"
"s_mov_b32 s71, 0x3f803f80\n"
"s_waitcnt lgkmcnt(8)\n"
"v_pk_add_f32 v[8:9], v[8:9], v[16:17]\n"
"v_pk_add_f32 v[10:11], v[10:11], v[18:19]\n"
"v_pk_add_f32 v[12:13], v[12:13], v[20:21]\n"
"v_pk_add_f32 v[14:15], v[14:15], v[22:23]\n"
"s_waitcnt lgkmcnt(6)\n"
"v_pk_add_f32 v[8:9], v[8:9], v[24:25]\n"
"v_pk_add_f32 v[10:11], v[10:11], v[26:27]\n"
"v_pk_add_f32 v[12:13], v[12:13], v[28:29]\n"
"v_pk_add_f32 v[14:15], v[14:15], v[30:31]\n"
"s_waitcnt lgkmcnt(4)\n"
"v_pk_add_f32 v[8:9], v[8:9], v[32:33]\n"
"v_pk_add_f32 v[10:11], v[10:11], v[34:35]\n"
"v_pk_add_f32 v[12:13], v[12:13], v[36:37]\n"
"v_pk_add_f32 v[14:15], v[14:15], v[38:39]\n"
"v_max3_f32 v3, v8, v9, v10\n"
"v_max3_f32 v3, v3, v11, v12\n"
"v_max3_f32 v3, v3, v13, v14\n"
"v_max_f32 v3, v3, v15\n"
"v_xor_b32 v7, 32, v4\n"
"v_xor_b32 v1, 48, v4\n"
"ds_read_b64_tr_b16 v[16:17], v7 offset:0\n"
"ds_read_b64_tr_b16 v[18:19], v1 offset:1536\n"
"ds_read_b64_tr_b16 v[20:21], v7 offset:49152\n"
"ds_read_b64_tr_b16 v[22:23], v1 offset:50688\n"
"v_max_f32_dpp v2, v3, v3 quad_perm:[1,0,3,2] row_mask:0xf bank_mask:0xf\n"
"v_max_f32_dpp v3, v3, v3 quad_perm:[1,0,3,2] row_mask:0xf bank_mask:0xf\n"
"v_xor_b32 v7, 64, v4\n"
"v_xor_b32 v1, 80, v4\n"
"ds_read_b64_tr_b16 v[24:25], v7 offset:0\n"
"ds_read_b64_tr_b16 v[26:27], v1 offset:1536\n"
"ds_read_b64_tr_b16 v[28:29], v7 offset:49152\n"
"ds_read_b64_tr_b16 v[30:31], v1 offset:50688\n"
"v_permlane16_swap_b32 v2, v3\n"
"v_max_f32 v2, v2, v3\n"
"v_mov_b32 v3, v2\n"
"s_nop 1\n"
"v_permlane32_swap_b32 v2, v3\n"
"v_max_f32 v2, v2, v3\n"
"v_pk_add_f32 v[8:9], v[8:9], v[2:3] op_sel_hi:[1,0] neg_lo:[0,1] neg_hi:[0,1]\n"
"v_pk_add_f32 v[10:11], v[10:11], v[2:3] op_sel_hi:[1,0] neg_lo:[0,1] neg_hi:[0,1]\n"
"v_pk_add_f32 v[12:13], v[12:13], v[2:3] op_sel_hi:[1,0] neg_lo:[0,1] neg_hi:[0,1]\n"
"v_pk_add_f32 v[14:15], v[14:15], v[2:3] op_sel_hi:[1,0] neg_lo:[0,1] neg_hi:[0,1]\n"
"v_exp_f32 v8, v8\n"
"v_exp_f32 v10, v10\n"
"v_exp_f32 v9, v9\n"
"v_exp_f32 v11, v11\n"
"v_exp_f32 v12, v12\n"
"v_exp_f32 v14, v14\n"
"v_exp_f32 v13, v13\n"
"v_exp_f32 v15, v15\n"
"v_cvt_pk_bf16_f32 v40, v8, v10\n"
"v_cvt_pk_bf16_f32 v41, v9, v11\n"
"v_cvt_pk_bf16_f32 v42, v12, v14\n"
"v_cvt_pk_bf16_f32 v43, v13, v15\n"
"v_mov_b64 v[8:9], 0\n"
"v_mov_b64 v[10:11], 0\n"
"v_mov_b64 v[12:13], 0\n"
"v_mov_b64 v[14:15], 0\n"
"s_waitcnt lgkmcnt(8)\n"
"v_smfmac_f32_16x16x64_bf16 v[8:11], v[40:43], v[120:127], v6\n"
"v_xor_b32 v7, 96, v4\n"
"v_xor_b32 v1, 112, v4\n"
"ds_read_b64_tr_b16 v[120:121], v7 offset:0\n"
"ds_read_b64_tr_b16 v[122:123], v1 offset:1536\n"
"ds_read_b64_tr_b16 v[124:125], v7 offset:49152\n"
"ds_read_b64_tr_b16 v[126:127], v1 offset:50688\n"
"s_cmp_eq_u32 s48, 0\n"
"s_cbranch_scc0 .Lr_118_%=\n"
"v_dot2_f32_bf16 v3, v40, s71, 0\n"
"v_dot2_f32_bf16 v3, v41, s71, v3\n"
"v_dot2_f32_bf16 v3, v42, s71, v3\n"
"v_dot2_f32_bf16 v3, v43, s71, v3\n"
"v_mov_b32 v32, v2\n"
"v_mov_b32 v34, s66\n"
"v_lshlrev_b32 v36, 13, v0\n"
"v_add_f32_dpp v33, v3, v3 quad_perm:[1,0,3,2] row_mask:0xf bank_mask:0xf\n"
"v_mov_b32 v35, v33\n"
"s_nop 1\n"
"v_permlane16_swap_b32 v33, v35\n"
"v_add_f32 v33, v33, v35\n"
"v_mov_b32 v35, v33\n"
"s_nop 1\n"
"v_permlane32_swap_b32 v33, v35\n"
"v_add_f32 v33, v33, v35\n"
"s_mov_b64 exec, 0x5555\n"
"buffer_store_dwordx3 v[32:34], v36, s[40:43], s64 offen offset:0 sc1\n"
"s_mov_b64 exec, -1\n"
".Lr_118_%=:\n"
"v_and_b32 v2, 15, v0\n"
"v_and_b32 v3, 48, v0\n"
"v_lshlrev_b32 v3, 15, v3\n"
"v_lshl_add_u32 v2, v2, 4, v3\n"
"v_mov_b32 v35, s66\n"
"v_mov_b32 v39, s66\n"
"s_waitcnt lgkmcnt(8)\n"
"v_smfmac_f32_16x16x64_bf16 v[12:15], v[40:43], v[16:23], v6\n"
"v_xor_b32 v7, 0, v5\n"
"v_xor_b32 v1, 16, v5\n"
"ds_read_b64_tr_b16 v[16:17], v7 offset:0\n"
"ds_read_b64_tr_b16 v[18:19], v1 offset:1536\n"
"ds_read_b64_tr_b16 v[20:21], v7 offset:49152\n"
"ds_read_b64_tr_b16 v[22:23], v1 offset:50688\n"
"v_add_f32 v32, v8, v9\n"
"v_add_f32 v36, v10, v11\n"
"v_mov_b64 v[8:9], 0\n"
"v_mov_b64 v[10:11], 0\n"
"s_waitcnt lgkmcnt(8)\n"
"s_nop 0\n"
"v_smfmac_f32_16x16x64_bf16 v[8:11], v[40:43], v[24:31], v6\n"
"v_xor_b32 v7, 32, v5\n"
"v_xor_b32 v1, 48, v5\n"
"ds_read_b64_tr_b16 v[24:25], v7 offset:0\n"
"ds_read_b64_tr_b16 v[26:27], v1 offset:1536\n"
"ds_read_b64_tr_b16 v[28:29], v7 offset:49152\n"
"ds_read_b64_tr_b16 v[30:31], v1 offset:50688\n"
"v_add_f32 v12, v12, v13\n"
"v_add_f32 v14, v14, v15\n"
"v_cvt_pk_bf16_f32 v32, v32, v12\n"
"v_cvt_pk_bf16_f32 v36, v36, v14\n"
"v_mov_b64 v[12:13], 0\n"
"v_mov_b64 v[14:15], 0\n"
"s_cmp_lg_u32 s48, 0\n"
"s_cbranch_scc0 .Lr_119_%=\n"
"s_mov_b32 m0, 768\n"
"s_mul_i32 s70, s48, 16\n"
"s_add_u32 s70, s70, 32\n"
"v_add_u32 v3, s70, v0\n"
"v_lshlrev_b32 v3, 7, v3\n"
"s_mul_i32 s70, s50, 30560\n"
"s_mov_b32 exec_lo, 0xffff\n"
"s_mov_b32 exec_hi, 0x0\n"
"buffer_load_dword v3, s[36:39], s70 offen lds\n"
"s_mov_b64 exec, -1\n"
".Lr_119_%=:\n"
"s_waitcnt lgkmcnt(8)\n"
"v_smfmac_f32_16x16x64_bf16 v[12:15], v[40:43], v[120:127], v6\n"
"v_xor_b32 v7, 64, v5\n"
"v_xor_b32 v1, 80, v5\n"
"ds_read_b64_tr_b16 v[120:121], v7 offset:0\n"
"ds_read_b64_tr_b16 v[122:123], v1 offset:1536\n"
"ds_read_b64_tr_b16 v[124:125], v7 offset:49152\n"
"ds_read_b64_tr_b16 v[126:127], v1 offset:50688\n"
"v_add_f32 v33, v8, v9\n"
"v_add_f32 v37, v10, v11\n"
"v_mov_b64 v[8:9], 0\n"
"v_mov_b64 v[10:11], 0\n"
"s_waitcnt lgkmcnt(8)\n"
"s_nop 0\n"
"v_smfmac_f32_16x16x64_bf16 v[8:11], v[40:43], v[16:23], v6\n"
"v_xor_b32 v7, 96, v5\n"
"v_xor_b32 v1, 112, v5\n"
"ds_read_b64_tr_b16 v[16:17], v7 offset:0\n"
"ds_read_b64_tr_b16 v[18:19], v1 offset:1536\n"
"ds_read_b64_tr_b16 v[20:21], v7 offset:49152\n"
"ds_read_b64_tr_b16 v[22:23], v1 offset:50688\n"
"v_add_f32 v12, v12, v13\n"
"v_add_f32 v14, v14, v15\n"
"v_cvt_pk_bf16_f32 v33, v33, v12\n"
"v_cvt_pk_bf16_f32 v37, v37, v14\n"
"v_mov_b64 v[12:13], 0\n"
"v_mov_b64 v[14:15], 0\n"
"s_waitcnt lgkmcnt(8)\n"
"s_nop 0\n"
"v_smfmac_f32_16x16x64_bf16 v[12:15], v[40:43], v[24:31], v6\n"
"v_xor_b32 v7, 0, v4\n"
"v_xor_b32 v1, 16, v4\n"
"ds_read_b64_tr_b16 v[24:25], v7 offset:256\n"
"ds_read_b64_tr_b16 v[26:27], v1 offset:1792\n"
"ds_read_b64_tr_b16 v[28:29], v7 offset:49408\n"
"ds_read_b64_tr_b16 v[30:31], v1 offset:50944\n"
"v_add_f32 v34, v8, v9\n"
"v_add_f32 v38, v10, v11\n"
"v_mov_b64 v[8:9], 0\n"
"v_mov_b64 v[10:11], 0\n"
"s_waitcnt lgkmcnt(8)\n"
"s_nop 0\n"
"v_smfmac_f32_16x16x64_bf16 v[8:11], v[40:43], v[120:127], v6\n"
"v_xor_b32 v7, 32, v4\n"
"v_xor_b32 v1, 48, v4\n"
"ds_read_b64_tr_b16 v[120:121], v7 offset:256\n"
"ds_read_b64_tr_b16 v[122:123], v1 offset:1792\n"
"ds_read_b64_tr_b16 v[124:125], v7 offset:49408\n"
"ds_read_b64_tr_b16 v[126:127], v1 offset:50944\n"
"v_add_f32 v12, v12, v13\n"
"v_add_f32 v14, v14, v15\n"
"v_cvt_pk_bf16_f32 v34, v34, v12\n"
"v_cvt_pk_bf16_f32 v38, v38, v14\n"
"v_mov_b64 v[12:13], 0\n"
"v_mov_b64 v[14:15], 0\n"
"s_add_u32 s70, s63, 0\n"
"buffer_store_dwordx4 v[32:35], v2, s[40:43], s70 offen offset:0 sc1\n"
"s_add_u32 s70, s63, 262144\n"
"buffer_store_dwordx4 v[36:39], v2, s[40:43], s70 offen offset:0 sc1\n"
"s_waitcnt lgkmcnt(8)\n"
"v_smfmac_f32_16x16x64_bf16 v[12:15], v[40:43], v[16:23], v6\n"
"v_xor_b32 v7, 64, v4\n"
"v_xor_b32 v1, 80, v4\n"
"ds_read_b64_tr_b16 v[16:17], v7 offset:256\n"
"ds_read_b64_tr_b16 v[18:19], v1 offset:1792\n"
"ds_read_b64_tr_b16 v[20:21], v7 offset:49408\n"
"ds_read_b64_tr_b16 v[22:23], v1 offset:50944\n"
"v_add_f32 v32, v8, v9\n"
"v_add_f32 v36, v10, v11\n"
"v_mov_b64 v[8:9], 0\n"
"v_mov_b64 v[10:11], 0\n"
"s_waitcnt lgkmcnt(8)\n"
"s_nop 0\n"
"v_smfmac_f32_16x16x64_bf16 v[8:11], v[40:43], v[24:31], v6\n"
"v_xor_b32 v7, 96, v4\n"
"v_xor_b32 v1, 112, v4\n"
"ds_read_b64_tr_b16 v[24:25], v7 offset:256\n"
"ds_read_b64_tr_b16 v[26:27], v1 offset:1792\n"
"ds_read_b64_tr_b16 v[28:29], v7 offset:49408\n"
"ds_read_b64_tr_b16 v[30:31], v1 offset:50944\n"
"v_add_f32 v12, v12, v13\n"
"v_add_f32 v14, v14, v15\n"
"v_cvt_pk_bf16_f32 v32, v32, v12\n"
"v_cvt_pk_bf16_f32 v36, v36, v14\n"
"v_mov_b64 v[12:13], 0\n"
"v_mov_b64 v[14:15], 0\n"
"s_waitcnt lgkmcnt(8)\n"
"s_nop 0\n"
"v_smfmac_f32_16x16x64_bf16 v[12:15], v[40:43], v[120:127], v6\n"
"v_lshl_add_u32 v7, s48, 7, v0\n"
"v_lshlrev_b32 v7, 4, v7\n"
"ds_read_b128 v[120:123], v7 offset:47104\n"
"ds_read_b128 v[124:127], v7 offset:48128\n"
"v_add_f32 v33, v8, v9\n"
"v_add_f32 v37, v10, v11\n"
"v_mov_b64 v[8:9], 0\n"
"v_mov_b64 v[10:11], 0\n"
"s_waitcnt lgkmcnt(6)\n"
"s_nop 0\n"
"v_smfmac_f32_16x16x64_bf16 v[8:11], v[40:43], v[16:23], v6\n"
"v_add_f32 v12, v12, v13\n"
"v_add_f32 v14, v14, v15\n"
"v_cvt_pk_bf16_f32 v33, v33, v12\n"
"v_cvt_pk_bf16_f32 v37, v37, v14\n"
"v_mov_b64 v[12:13], 0\n"
"v_mov_b64 v[14:15], 0\n"
"s_waitcnt lgkmcnt(2)\n"
"s_nop 0\n"
"v_smfmac_f32_16x16x64_bf16 v[12:15], v[40:43], v[24:31], v6\n"
"v_add_f32 v34, v8, v9\n"
"v_add_f32 v38, v10, v11\n"
"s_nop 5\n"
"v_add_f32 v12, v12, v13\n"
"v_add_f32 v14, v14, v15\n"
"v_cvt_pk_bf16_f32 v34, v34, v12\n"
"v_cvt_pk_bf16_f32 v38, v38, v14\n"
"s_add_u32 s70, s63, 32768\n"
"buffer_store_dwordx4 v[32:35], v2, s[40:43], s70 offen offset:0 sc1\n"
"s_add_u32 s70, s63, 294912\n"
"buffer_store_dwordx4 v[36:39], v2, s[40:43], s70 offen offset:0 sc1\n"
"s_waitcnt lgkmcnt(0)\n"
"s_barrier\n"
"s_cmp_lg_u32 s48, 0\n"
"s_cbranch_scc0 .Lr_120_%=\n"
"s_lshl_b32 s70, s48, 4\n"
"s_sub_u32 s70, s70, 16\n"
"v_lshrrev_b32 v30, 3, v0\n"
"v_add_u32 v30, s70, v30\n"
"v_mul_u32_u24 v31, 43, v30\n"
"v_lshrrev_b32 v31, 7, v31\n"
"v_mad_i32_i24 v30, v31, -3, v30\n"
"v_lshrrev_b32 v28, 1, v31\n"
"v_and_b32 v28, 4, v28\n"
"v_and_or_b32 v28, v31, 2, v28\n"
"v_xor_b32 v28, v0, v28\n"
"v_and_b32 v28, 7, v28\n"
"v_lshl_add_u32 v28, v30, 3, v28\n"
"v_lshlrev_b32 v28, 4, v28\n"
"v_and_b32 v30, 1, v31\n"
"v_lshrrev_b32 v31, 1, v31\n"
"v_lshl_add_u32 v31, v30, 4, v31\n"
"v_mul_u32_u24 v31, 12288, v31\n"
"v_add_u32 v28, v28, v31\n"
"s_add_u32 s71, s70, 8\n"
"v_lshrrev_b32 v30, 3, v0\n"
"v_add_u32 v30, s71, v30\n"
"v_mul_u32_u24 v31, 43, v30\n"
"v_lshrrev_b32 v31, 7, v31\n"
"v_mad_i32_i24 v30, v31, -3, v30\n"
"v_lshrrev_b32 v29, 1, v31\n"
"v_and_b32 v29, 4, v29\n"
"v_and_or_b32 v29, v31, 2, v29\n"
"v_xor_b32 v29, v0, v29\n"
"v_and_b32 v29, 7, v29\n"
"v_lshl_add_u32 v29, v30, 3, v29\n"
"v_lshlrev_b32 v29, 4, v29\n"
"v_and_b32 v30, 1, v31\n"
"v_lshrrev_b32 v31, 1, v31\n"
"v_lshl_add_u32 v31, v30, 4, v31\n"
"v_mul_u32_u24 v31, 12288, v31\n"
"v_add_u32 v29, v29, v31\n"
"s_lshl_b32 s70, s70, 7\n"
"s_add_u32 m0, s70, 47104\n"
"s_mov_b32 s71, 0\n"
"buffer_load_dwordx4 v28, s[32:35], s71 offen offset:0 nt lds\n"
"s_add_u32 m0, m0, 1024\n"
"s_nop 0\n"
"buffer_load_dwordx4 v29, s[32:35], s71 offen offset:0 nt lds\n"
"s_add_u32 m0, s70, 53248\n"
"s_mov_b32 s71, 98304\n"
"buffer_load_dwordx4 v28, s[32:35], s71 offen offset:0 nt lds\n"
"s_add_u32 m0, m0, 1024\n"
"s_nop 0\n"
"buffer_load_dwordx4 v29, s[32:35], s71 offen offset:0 nt lds\n"
"s_add_u32 m0, s70, 59392\n"
"s_mov_b32 s71, 393216\n"
"buffer_load_dwordx4 v28, s[32:35], s71 offen offset:0 nt lds\n"
"s_add_u32 m0, m0, 1024\n"
"s_nop 0\n"
"buffer_load_dwordx4 v29, s[32:35], s71 offen offset:0 nt lds\n"
"s_add_u32 m0, s70, 65536\n"
"s_mov_b32 s71, 491520\n"
"buffer_load_dwordx4 v28, s[32:35], s71 offen offset:0 nt lds\n"
"s_add_u32 m0, m0, 1024\n"
"s_nop 0\n"
"buffer_load_dwordx4 v29, s[32:35], s71 offen offset:0 nt lds\n"
"s_add_u32 m0, s70, 71680\n"
"s_mov_b32 s71, 786432\n"
"buffer_load_dwordx4 v28, s[32:35], s71 offen offset:0 nt lds\n"
"s_add_u32 m0, m0, 1024\n"
"s_nop 0\n"
"buffer_load_dwordx4 v29, s[32:35], s71 offen offset:0 nt lds\n"
"s_add_u32 m0, s70, 77824\n"
"s_mov_b32 s71, 884736\n"
"buffer_load_dwordx4 v28, s[32:35], s71 offen offset:0 nt lds\n"
"s_add_u32 m0, m0, 1024\n"
"s_nop 0\n"
"buffer_load_dwordx4 v29, s[32:35], s71 offen offset:0 nt lds\n"
"s_mov_b32 m0, 768\n"
"s_mul_i32 s70, s48, 16\n"
"s_add_u32 s70, s70, 80\n"
"v_add_u32 v29, s70, v0\n"
"v_lshlrev_b32 v29, 7, v29\n"
"s_mul_i32 s70, s50, 30560\n"
"s_mov_b32 exec_lo, 0xffff\n"
"s_mov_b32 exec_hi, 0x0\n"
"buffer_load_dword v29, s[36:39], s70 offen lds\n"
"s_mov_b64 exec, -1\n"
".Lr_120_%=:\n"
"v_bfe_i32 v3, v0, 3, 1\n"
"v_not_b32 v2, v3\n"
"v_bfe_u32 v1, v0, 2, 1\n"
"v_and_b32 v4, 1, v0\n"
"v_lshl_add_u32 v1, v1, 1, v4\n"
"v_mul_u32_u24 v1, 0x505, v1\n"
"v_mul_u32_u24 v5, 0x5050, v4\n"
"v_add_u32 v1, v1, v5\n"
"v_lshrrev_b32 v5, 2, v0\n"
"v_add_u32 v5, s48, v5\n"
"v_and_b32 v5, 3, v5\n"
"v_lshl_add_u32 v4, v5, 4, v4\n"
"v_and_b32 v5, 2, v0\n"
"v_lshl_add_u32 v4, v5, 2, v4\n"
"v_lshrrev_b32 v5, 4, v0\n"
"v_lshl_add_u32 v4, v5, 1, v4\n"
"v_lshlrev_b32 v4, 4, v4\n"
"v_add_u32 v4, 5120, v4\n"
"v_lshlrev_b32 v7, 4, v0\n"
"s_mul_i32 s70, s48, 208\n"
"v_lshl_add_u32 v5, v0, 2, s70\n"
"v_mov_b64 v[36:37], 0\n"
"s_sleep 20\n"
"buffer_load_dwordx3 v[24:26], v4, s[40:43], s56 offen offset:0 sc1\n"
"v_lshrrev_b32 v41, 2, v0\n"
"v_and_b32 v42, 12, v41\n"
"v_add_u32 v41, v41, v42\n"
"v_lshrrev_b32 v42, 1, v41\n"
"v_and_b32 v42, 4, v42\n"
"v_and_or_b32 v42, v41, 2, v42\n"
"v_lshlrev_b32 v42, 1, v42\n"
"v_and_or_b32 v42, v0, 3, v42\n"
"v_lshlrev_b32 v42, 3, v42\n"
"v_mul_u32_u24 v41, 384, v41\n"
"v_add_u32 v40, v41, v42\n"
"s_mul_i32 s70, s48, 3\n"
"s_add_u32 s70, s70, 0\n"
"s_and_b32 s71, s70, 3\n"
"s_lshl_b32 s71, s71, 5\n"
"s_lshr_b32 s70, s70, 2\n"
"s_lshl_b32 s70, s70, 7\n"
"s_add_u32 s70, s70, 47104\n"
"v_xor_b32 v41, s71, v40\n"
"v_add_u32 v41, s70, v41\n"
"s_mul_i32 s70, s48, 3\n"
"s_add_u32 s70, s70, 1\n"
"s_and_b32 s71, s70, 3\n"
"s_lshl_b32 s71, s71, 5\n"
"s_lshr_b32 s70, s70, 2\n"
"s_lshl_b32 s70, s70, 7\n"
"s_add_u32 s70, s70, 47104\n"
"v_xor_b32 v42, s71, v40\n"
"v_add_u32 v42, s70, v42\n"
"s_mul_i32 s70, s48, 3\n"
"s_add_u32 s70, s70, 2\n"
"s_and_b32 s71, s70, 3\n"
"s_lshl_b32 s71, s71, 5\n"
"s_lshr_b32 s70, s70, 2\n"
"s_lshl_b32 s70, s70, 7\n"
"s_add_u32 s70, s70, 47104\n"
"v_xor_b32 v43, s71, v40\n"
"v_add_u32 v43, s70, v43\n"
"s_sleep 2\n"
"buffer_load_dwordx4 v[8:11], v7, s[40:43], s62 offen offset:0 sc1\n"
"buffer_load_dwordx4 v[12:15], v7, s[40:43], s62 offen offset:1024 sc1\n"
"buffer_load_dwordx4 v[16:19], v7, s[40:43], s62 offen offset:2048 sc1\n"
"buffer_load_dwordx4 v[20:23], v7, s[40:43], s62 offen offset:3072 sc1\n"
"s_barrier\n"
"s_cmp_lg_u32 s48, 0\n"
"s_cbranch_scc0 .Lr_121_%=\n"
"s_mov_b32 m0, 768\n"
"s_mul_i32 s70, s48, 32\n"
"s_add_u32 s70, s70, 112\n"
"v_add_u32 v29, s70, v0\n"
"v_lshlrev_b32 v29, 7, v29\n"
"s_mul_i32 s70, s50, 30560\n"
"s_mov_b32 exec_lo, 0xffffffff\n"
"s_mov_b32 exec_hi, 0x0\n"
"buffer_load_dword v29, s[36:39], s70 offen lds\n"
"s_mov_b64 exec, -1\n"
"s_sub_u32 s70, s48, 1\n"
"s_mul_i32 s71, s70, 81920\n"
"s_mul_i32 s70, s70, 10240\n"
"s_movk_i32 s72, 1120\n"
"v_mul_u32_u24 v28, 205, v0\n"
"v_lshlrev_b32 v29, 4, v0\n"
"s_add_u32 m0, s70, 83968\n"
"v_add_u32 v30, 0, v28\n"
"v_lshrrev_b32 v30, 11, v30\n"
"v_mad_u32_u24 v30, v30, s72, v29\n"
"buffer_load_dwordx4 v30, s[36:39], s71 offen offset:0 nt lds\n"
"v_add_u32 v30, 13120, v28\n"
"v_lshrrev_b32 v30, 11, v30\n"
"v_mad_u32_u24 v30, v30, s72, v29\n"
"buffer_load_dwordx4 v30, s[36:39], s71 offen offset:1024 nt lds\n"
"v_add_u32 v30, 26240, v28\n"
"v_lshrrev_b32 v30, 11, v30\n"
"v_mad_u32_u24 v30, v30, s72, v29\n"
"buffer_load_dwordx4 v30, s[36:39], s71 offen offset:2048 nt lds\n"
".Lr_121_%=:\n"
".Lgb_122_%=:\n"
"s_waitcnt vmcnt(8)\n"
"s_cmp_eq_u32 s48, 0\n"
"s_cbranch_scc0 .Lr_125_%=\n"
"s_waitcnt vmcnt(4)\n"
".Lr_125_%=:\n"
"v_max_f32_dpp v27, v24, v24 row_ror:1 row_mask:0xf bank_mask:0xf\n"
"s_nop 1\n"
"v_max_f32_dpp v27, v27, v27 row_ror:2 row_mask:0xf bank_mask:0xf\n"
"s_nop 1\n"
"v_max_f32_dpp v27, v27, v27 row_ror:4 row_mask:0xf bank_mask:0xf\n"
"s_nop 1\n"
"v_max_f32_dpp v27, v27, v27 row_ror:8 row_mask:0xf bank_mask:0xf\n"
"s_nop 1\n"
"v_max_f32_dpp v27, v27, v27 row_bcast:15 row_mask:0xa bank_mask:0xf\n"
"s_nop 1\n"
"v_max_f32_dpp v27, v27, v27 row_bcast:31 row_mask:0xc bank_mask:0xf\n"
"s_nop 0\n"
"v_readlane_b32 s70, v27, 63\n"
"s_nop 1\n"
"v_subrev_f32 v24, s70, v24\n"
"v_exp_f32 v24, v24\n"
"s_nop 0\n"
"v_cvt_pk_bf16_f32 v24, v24, 0\n"
"v_lshlrev_b32 v27, 16, v24\n"
"v_mul_f32 v27, v27, v25\n"
"v_and_b32_dpp v28, v24, v2 row_newbcast:0 row_mask:0xf bank_mask:0xf\n"
"v_and_b32_dpp v29, v24, v3 row_newbcast:0 row_mask:0xf bank_mask:0xf\n"
"v_add_f32_dpp v27, v27, v27 row_ror:1 row_mask:0xf bank_mask:0xf\n"
"v_and_b32_dpp v30, v24, v2 row_newbcast:1 row_mask:0xf bank_mask:0xf\n"
"v_and_b32_dpp v31, v24, v3 row_newbcast:1 row_mask:0xf bank_mask:0xf\n"
"v_add_f32_dpp v27, v27, v27 row_ror:2 row_mask:0xf bank_mask:0xf\n"
"v_and_b32_dpp v32, v24, v2 row_newbcast:2 row_mask:0xf bank_mask:0xf\n"
"v_and_b32_dpp v33, v24, v3 row_newbcast:2 row_mask:0xf bank_mask:0xf\n"
"v_add_f32_dpp v27, v27, v27 row_ror:4 row_mask:0xf bank_mask:0xf\n"
"v_and_b32_dpp v34, v24, v2 row_newbcast:3 row_mask:0xf bank_mask:0xf\n"
"v_and_b32_dpp v35, v24, v3 row_newbcast:3 row_mask:0xf bank_mask:0xf\n"
"v_add_f32_dpp v27, v27, v27 row_ror:8 row_mask:0xf bank_mask:0xf\n"
"s_nop 1\n"
"v_add_f32_dpp v27, v27, v27 row_bcast:15 row_mask:0xa bank_mask:0xf\n"
"s_nop 1\n"
"v_add_f32_dpp v27, v27, v27 row_bcast:31 row_mask:0xc bank_mask:0xf\n"
"v_rcp_f32 v27, v27\n"
"s_nop 0\n"
"v_readlane_b32 s70, v27, 63\n"
"s_waitcnt vmcnt(6)\n"
"s_cmp_eq_u32 s48, 0\n"
"s_cbranch_scc0 .Lr_126_%=\n"
"s_waitcnt vmcnt(2)\n"
".Lr_126_%=:\n"
"v_smfmac_f32_16x16x64_bf16 v[36:39], v[28:31], v[8:15], v1\n"
"s_waitcnt vmcnt(4)\n"
"s_cmp_eq_u32 s48, 0\n"
"s_cbranch_scc0 .Lr_127_%=\n"
"s_waitcnt vmcnt(0)\n"
".Lr_127_%=:\n"
"v_smfmac_f32_16x16x64_bf16 v[36:39], v[32:35], v[16:23], v1\n"
"v_bitop3_b32 v40, v11, v15, s66 bitop3:0x7e\n"
"v_bitop3_b32 v40, v19, v40, s66 bitop3:0xde\n"
"v_bitop3_b32 v40, v23, v40, s66 bitop3:0xde\n"
"v_bitop3_b32 v40, v26, v40, s66 bitop3:0xde\n"
"v_cmp_ne_u32 vcc, 0, v40\n"
"s_mov_b32 exec_hi, 0xffff\n"
"s_nop 1\n"
"v_mul_f32 v36, s70, v36\n"
"v_mul_f32 v37, s70, v37\n"
"v_cvt_pk_bf16_f32 v36, v36, v37\n"
"ds_write_b32 v5, v36 offset:3072\n"
"s_mov_b32 exec_hi, -1\n"
"s_cbranch_vccnz .Lgr_123_%=\n"
".Lgo_124_%=:\n"
"v_and_b32 v7, 3, v0\n"
"v_mul_u32_u24 v7, 208, v7\n"
"v_and_b32 v1, 48, v0\n"
"v_add_u32 v7, v7, v1\n"
"s_waitcnt lgkmcnt(0)\n"
"s_barrier\n"
"ds_read_b128 v[8:11], v7 offset:3072\n"
"ds_read_b128 v[12:15], v7 offset:3136\n"
"ds_read_b128 v[16:19], v7 offset:3200\n"
"ds_read_b64_tr_b16 v[2:3], v41 offset:0\n"
"ds_read_b64_tr_b16 v[4:5], v41 offset:1536\n"
"ds_read_b64_tr_b16 v[32:33], v41 offset:12288\n"
"ds_read_b64_tr_b16 v[34:35], v41 offset:13824\n"
"ds_read_b64_tr_b16 v[36:37], v41 offset:24576\n"
"ds_read_b64_tr_b16 v[38:39], v41 offset:26112\n"
"s_add_u32 s66, s54, 5\n"
"s_lshl2_add_u32 s70, s50, s48\n"
"s_lshl_b32 s70, s70, 7\n"
"s_add_u32 s70, s70, s57\n"
"v_lshlrev_b32 v1, 3, v0\n"
"s_waitcnt lgkmcnt(4)\n"
"v_mfma_f32_16x16x32_bf16 v[20:23], v[8:11], v[2:5], 0\n"
"ds_read_b64_tr_b16 v[2:3], v42 offset:0\n"
"ds_read_b64_tr_b16 v[4:5], v42 offset:1536\n"
"s_waitcnt lgkmcnt(4)\n"
"v_mfma_f32_16x16x32_bf16 v[20:23], v[12:15], v[32:35], v[20:23]\n"
"ds_read_b64_tr_b16 v[32:33], v42 offset:12288\n"
"ds_read_b64_tr_b16 v[34:35], v42 offset:13824\n"
"s_waitcnt lgkmcnt(4)\n"
"v_mfma_f32_16x16x32_bf16 v[20:23], v[16:19], v[36:39], v[20:23]\n"
"ds_read_b64_tr_b16 v[36:37], v42 offset:24576\n"
"ds_read_b64_tr_b16 v[38:39], v42 offset:26112\n"
"s_waitcnt lgkmcnt(4)\n"
"v_mfma_f32_16x16x32_bf16 v[24:27], v[8:11], v[2:5], 0\n"
"ds_read_b64_tr_b16 v[2:3], v43 offset:0\n"
"ds_read_b64_tr_b16 v[4:5], v43 offset:1536\n"
"s_waitcnt lgkmcnt(4)\n"
"v_mfma_f32_16x16x32_bf16 v[24:27], v[12:15], v[32:35], v[24:27]\n"
"ds_read_b64_tr_b16 v[32:33], v43 offset:12288\n"
"ds_read_b64_tr_b16 v[34:35], v43 offset:13824\n"
"v_pk_add_f32 v[20:21], v[20:21], v[22:23]\n"
"v_add_f32 v20, v20, v21\n"
"s_waitcnt lgkmcnt(4)\n"
"v_mfma_f32_16x16x32_bf16 v[24:27], v[16:19], v[36:39], v[24:27]\n"
"ds_read_b64_tr_b16 v[36:37], v43 offset:24576\n"
"ds_read_b64_tr_b16 v[38:39], v43 offset:26112\n"
"s_waitcnt lgkmcnt(4)\n"
"v_mfma_f32_16x16x32_bf16 v[28:31], v[8:11], v[2:5], 0\n"
"s_waitcnt lgkmcnt(2)\n"
"v_mfma_f32_16x16x32_bf16 v[28:31], v[12:15], v[32:35], v[28:31]\n"
"v_pk_add_f32 v[24:25], v[24:25], v[26:27]\n"
"v_add_f32 v24, v24, v25\n"
"s_waitcnt lgkmcnt(0)\n"
"v_mfma_f32_16x16x32_bf16 v[28:31], v[16:19], v[36:39], v[28:31]\n"
"v_mov_b32 v35, s66\n"
"s_nop 6\n"
"v_pk_add_f32 v[28:29], v[28:29], v[30:31]\n"
"v_add_f32 v28, v28, v29\n"
"v_mov_b32_dpp v21, v20 quad_perm:[1,1,3,3] row_mask:0xf bank_mask:0xf\n"
"v_mov_b32_dpp v25, v24 quad_perm:[1,1,3,3] row_mask:0xf bank_mask:0xf\n"
"v_mov_b32_dpp v29, v28 quad_perm:[1,1,3,3] row_mask:0xf bank_mask:0xf\n"
"s_mov_b64 exec, 0x5555\n"
"v_cvt_pk_bf16_f32 v32, v20, v21\n"
"v_cvt_pk_bf16_f32 v33, v24, v25\n"
"v_cvt_pk_bf16_f32 v34, v28, v29\n"
"buffer_store_dwordx4 v[32:35], v1, s[40:43], s70 offen offset:0 sc1\n"
"s_mov_b64 exec, -1\n"
"s_cmp_lg_u32 s48, 0\n"
"s_cbranch_scc0 .Lr_128_%=\n"
"s_sub_u32 s70, s48, 1\n"
"s_mul_i32 s71, s70, 81920\n"
"s_mul_i32 s70, s70, 10240\n"
"s_movk_i32 s72, 1120\n"
"v_mul_u32_u24 v1, 205, v0\n"
"v_lshlrev_b32 v2, 4, v0\n"
"s_add_u32 m0, s70, 83968\n"
"v_add_u32 v3, 39360, v1\n"
"v_lshrrev_b32 v3, 11, v3\n"
"v_mad_u32_u24 v3, v3, s72, v2\n"
"buffer_load_dwordx4 v3, s[36:39], s71 offen offset:3072 nt lds\n"
"s_add_u32 m0, s70, 88064\n"
"s_add_u32 s71, s71, 4096\n"
"v_add_u32 v3, 52480, v1\n"
"v_lshrrev_b32 v3, 11, v3\n"
"v_mad_u32_u24 v3, v3, s72, v2\n"
"buffer_load_dwordx4 v3, s[36:39], s71 offen offset:0 nt lds\n"
"v_add_u32 v3, 65600, v1\n"
"v_lshrrev_b32 v3, 11, v3\n"
"v_mad_u32_u24 v3, v3, s72, v2\n"
"buffer_load_dwordx4 v3, s[36:39], s71 offen offset:1024 nt lds\n"
"v_add_u32 v3, 78720, v1\n"
"v_lshrrev_b32 v3, 11, v3\n"
"v_mad_u32_u24 v3, v3, s72, v2\n"
"buffer_load_dwordx4 v3, s[36:39], s71 offen offset:2048 nt lds\n"
"v_add_u32 v3, 91840, v1\n"
"v_lshrrev_b32 v3, 11, v3\n"
"v_mad_u32_u24 v3, v3, s72, v2\n"
"buffer_load_dwordx4 v3, s[36:39], s71 offen offset:3072 nt lds\n"
"s_add_u32 m0, s70, 92160\n"
"s_add_u32 s71, s71, 4096\n"
"v_add_u32 v3, 104960, v1\n"
"v_lshrrev_b32 v3, 11, v3\n"
"v_mad_u32_u24 v3, v3, s72, v2\n"
"buffer_load_dwordx4 v3, s[36:39], s71 offen offset:0 nt lds\n"
"v_add_u32 v3, 118080, v1\n"
"v_lshrrev_b32 v3, 11, v3\n"
"v_mad_u32_u24 v3, v3, s72, v2\n"
"buffer_load_dwordx4 v3, s[36:39], s71 offen offset:1024 nt lds\n"
".Lr_128_%=:\n"
"s_mul_i32 s75, s49, 160\n"
"s_add_u32 s75, s75, s58\n"
"s_lshl_b32 s78, s48, 5\n"
"s_add_u32 s75, s75, s78\n"
"s_add_u32 s75, s75, 131072\n"
"s_mov_b32 s78, 0x55555555\n"
"s_cmp_eq_u32 s48, 3\n"
"s_cselect_b32 s79, s78, 0\n"
"v_lshrrev_b32 v2, 2, v0\n"
"v_and_b32 v43, 12, v2\n"
"v_add_u32 v2, v2, v43\n"
"v_mul_u32_u24 v2, 20, v2\n"
"v_and_b32 v43, 3, v0\n"
"v_add_lshl_u32 v43, v43, v2, 3\n"
"s_lshl_b32 s70, s48, 5\n"
"s_add_u32 s70, s70, 83968\n"
"v_add_u32 v43, s70, v43\n"
"s_cmp_eq_u32 s48, 0\n"
"s_cbranch_scc0 .Lr_129_%=\n"
"v_and_b32 v38, 15, v0\n"
"v_lshlrev_b32 v38, 1, v38\n"
"s_mov_b32 s70, 0x44ee44\n"
"v_bfe_u32 v1, s70, v38, 2\n"
"s_mov_b32 s70, 0xf5a0a0\n"
"v_bfe_u32 v38, s70, v38, 2\n"
"v_mul_u32_u24 v1, 5, v1\n"
"v_lshlrev_b32 v2, 2, v38\n"
"v_lshlrev_b32 v1, v2, v1\n"
"v_lshlrev_b32 v38, v38, 1\n"
"v_bfe_i32 v2, v38, 0, 1\n"
"v_and_b32 v2, 0x3f80, v2\n"
"v_bfe_i32 v3, v38, 1, 1\n"
"v_and_b32 v3, 0x3f80, v3\n"
"v_bfe_i32 v4, v38, 2, 1\n"
"v_and_b32 v4, 0x3f80, v4\n"
"v_bfe_i32 v5, v38, 3, 1\n"
"v_and_b32 v5, 0x3f80, v5\n"
"v_and_b32 v7, 15, v0\n"
"v_bfe_u32 v38, v0, 4, 1\n"
"v_lshl_add_u32 v7, v7, 1, v38\n"
"v_and_b32 v38, 32, v0\n"
"v_add_lshl_u32 v7, v7, v38, 4\n"
"v_mov_b64 v[28:29], 0\n"
"v_mov_b64 v[30:31], 0\n"
"v_bfe_u32 v38, v0, 2, 2\n"
"v_lshrrev_b32 v36, 4, v0\n"
"v_mad_u32_u24 v38, v38, 3, v36\n"
"v_and_b32 v36, 3, v0\n"
"v_lshl_add_u32 v36, v38, 3, v36\n"
"v_lshlrev_b32 v36, 2, v36\n"
"v_lshrrev_b32 v42, 4, v0\n"
"v_and_b32 v38, 1, v0\n"
"v_lshl_add_u32 v42, v42, 1, v38\n"
"v_lshlrev_b32 v42, 4, v42\n"
".Lr_129_%=:\n"
"s_cmp_lg_u32 s48, 0\n"
"s_cbranch_scc0 .Lr_130_%=\n"
"s_waitcnt vmcnt(3)\n"
".Lr_130_%=:\n"
"s_barrier\n"
"s_cmp_eq_u32 s48, 0\n"
"s_cbranch_scc0 .Lr_131_%=\n"
"buffer_load_dwordx4 v[8:11], v7, s[40:43], s57 offen offset:0 sc1\n"
"buffer_load_dwordx4 v[12:15], v7, s[40:43], s57 offen offset:1024 sc1\n"
"buffer_load_dwordx4 v[16:19], v7, s[40:43], s57 offen offset:2048 sc1\n"
"buffer_load_dwordx4 v[20:23], v7, s[40:43], s57 offen offset:3072 sc1\n"
".Lr_131_%=:\n"
"s_cmp_lg_u32 s48, 0\n"
"s_cbranch_scc0 .Lr_132_%=\n"
"s_cmp_eq_u32 s48, 3\n"
"s_cbranch_scc0 .Lr_133_%=\n"
"s_lshr_b32 s70, s2, 3\n"
"s_lshl_b32 s70, s70, 11\n"
"v_lshlrev_b32 v1, 7, v0\n"
"s_mov_b32 m0, 3968\n"
"s_mov_b64 exec, 0xffff\n"
"buffer_load_dword v1, s[44:47], s70 offen lds\n"
"s_mov_b64 exec, -1\n"
".Lr_133_%=:\n"
"v_lshrrev_b32 v42, 4, v0\n"
"v_and_b32 v38, 1, v0\n"
"v_lshl_add_u32 v42, v42, 1, v38\n"
"v_lshlrev_b32 v42, 4, v42\n"
"s_waitcnt vmcnt(1)\n"
"s_bitcmp1_b32 6, s48\n"
"s_cbranch_scc0 .Lr_134_%=\n"
"s_waitcnt vmcnt(0)\n"
".Lr_134_%=:\n"
".Lr_132_%=:\n"
"s_barrier\n"
"s_cmp_lg_u32 s48, 0\n"
"s_cbranch_scc0 .Lr_135_%=\n"
"ds_read_b64_tr_b16 v[8:9], v43 offset:0\n"
"ds_read_b64_tr_b16 v[10:11], v43 offset:640\n"
"ds_read_b64_tr_b16 v[12:13], v43 offset:5120\n"
"ds_read_b64_tr_b16 v[14:15], v43 offset:5760\n"
"ds_read_b64_tr_b16 v[16:17], v43 offset:10240\n"
"ds_read_b64_tr_b16 v[18:19], v43 offset:10880\n"
"ds_read_b64_tr_b16 v[20:21], v43 offset:15360\n"
"ds_read_b64_tr_b16 v[22:23], v43 offset:16000\n"
"ds_read_b64_tr_b16 v[24:25], v43 offset:20480\n"
"ds_read_b64_tr_b16 v[26:27], v43 offset:21120\n"
"ds_read_b64_tr_b16 v[28:29], v43 offset:25600\n"
"ds_read_b64_tr_b16 v[30:31], v43 offset:26240\n"
".Lr_135_%=:\n"
"s_cmp_eq_u32 s48, 0\n"
"s_cbranch_scc0 .Lr_136_%=\n"
".Lgb_137_%=:\n"
"s_waitcnt vmcnt(2)\n"
"v_smfmac_f32_16x16x64_bf16 v[28:31], v[2:5], v[8:15], v1\n"
"s_waitcnt vmcnt(0)\n"
"v_smfmac_f32_16x16x64_bf16 v[28:31], v[2:5], v[16:23], v1\n"
"v_bitop3_b32 v38, v11, v15, s66 bitop3:0x7e\n"
"v_bitop3_b32 v38, v19, v38, s66 bitop3:0xde\n"
"v_bitop3_b32 v38, v23, v38, s66 bitop3:0xde\n"
"v_cmp_ne_u32 vcc, 0, v38\n"
"s_nop 3\n"
"v_cvt_pk_bf16_f32 v40, v28, v29\n"
"v_cvt_pk_bf16_f32 v41, v30, v31\n"
"s_mov_b32 exec_hi, 0xffff\n"
"ds_write2_b32 v36, v40, v41 offset0:96 offset1:100\n"
"s_mov_b64 exec, -1\n"
"s_cbranch_vccnz .Lgr_138_%=\n"
".Lgo_139_%=:\n"
"ds_read_b64_tr_b16 v[8:9], v43 offset:0\n"
"ds_read_b64_tr_b16 v[10:11], v43 offset:640\n"
"ds_read_b64_tr_b16 v[12:13], v43 offset:5120\n"
"ds_read_b64_tr_b16 v[14:15], v43 offset:5760\n"
"ds_read_b64_tr_b16 v[16:17], v43 offset:10240\n"
"ds_read_b64_tr_b16 v[18:19], v43 offset:10880\n"
"ds_read_b64_tr_b16 v[20:21], v43 offset:15360\n"
"ds_read_b64_tr_b16 v[22:23], v43 offset:16000\n"
"ds_read_b64_tr_b16 v[24:25], v43 offset:20480\n"
"ds_read_b64_tr_b16 v[26:27], v43 offset:21120\n"
"ds_read_b64_tr_b16 v[28:29], v43 offset:25600\n"
"ds_read_b64_tr_b16 v[30:31], v43 offset:26240\n"
"s_waitcnt lgkmcnt(12)\n"
".Lr_136_%=:\n"
"s_barrier\n"
"v_mov_b32 v7, v43\n"
"ds_read_b128 v[32:35], v42 offset:384\n"
"ds_read_b128 v[36:39], v42 offset:512\n"
"ds_read_b128 v[40:43], v42 offset:640\n"
"v_mov_b64 v[2:3], 0\n"
"v_and_b32 v1, 15, v0\n"
"v_and_b32 v4, 32, v0\n"
"v_lshl_add_u32 v1, v1, 1, v4\n"
"v_and_b32 v4, 16, v0\n"
"v_lshl_add_u32 v1, v4, 9, v1\n"
"s_waitcnt lgkmcnt(2)\n"
"v_smfmac_f32_16x16x64_bf16 v[2:5], v[32:35], v[8:15], v6\n"
"s_waitcnt lgkmcnt(1)\n"
"v_smfmac_f32_16x16x64_bf16 v[2:5], v[36:39], v[16:23], v6\n"
"s_waitcnt lgkmcnt(0)\n"
"v_smfmac_f32_16x16x64_bf16 v[2:5], v[40:43], v[24:31], v6\n"
"s_cmp_eq_u32 s48, 3\n"
"s_cbranch_scc0 .Lr_140_%=\n"
"ds_read_b64_tr_b16 v[8:9], v7 offset:32\n"
"ds_read_b64_tr_b16 v[10:11], v7 offset:672\n"
"ds_read_b64_tr_b16 v[12:13], v7 offset:5152\n"
"ds_read_b64_tr_b16 v[14:15], v7 offset:5792\n"
"ds_read_b64_tr_b16 v[16:17], v7 offset:10272\n"
"ds_read_b64_tr_b16 v[18:19], v7 offset:10912\n"
"ds_read_b64_tr_b16 v[20:21], v7 offset:15392\n"
"ds_read_b64_tr_b16 v[22:23], v7 offset:16032\n"
"ds_read_b64_tr_b16 v[24:25], v7 offset:20512\n"
"ds_read_b64_tr_b16 v[26:27], v7 offset:21152\n"
"ds_read_b64_tr_b16 v[28:29], v7 offset:25632\n"
"ds_read_b64_tr_b16 v[30:31], v7 offset:26272\n"
".Lr_140_%=:\n"
"s_nop 5\n"
"v_add_f32 v7, v2, v3\n"
"s_cmp_eq_u32 s48, 3\n"
"s_cbranch_scc0 .Lr_141_%=\n"
"v_mov_b64 v[2:3], 0\n"
"s_waitcnt lgkmcnt(8)\n"
"s_nop 0\n"
"v_smfmac_f32_16x16x64_bf16 v[2:5], v[32:35], v[8:15], v6\n"
"s_waitcnt lgkmcnt(4)\n"
"v_smfmac_f32_16x16x64_bf16 v[2:5], v[36:39], v[16:23], v6\n"
"s_waitcnt lgkmcnt(0)\n"
"v_smfmac_f32_16x16x64_bf16 v[2:5], v[40:43], v[24:31], v6\n"
"s_nop 7\n"
"v_add_f32_dpp v7, v2, v3 quad_perm:[0,1,2,3] row_mask:0xc bank_mask:0xf\n"
".Lr_141_%=:\n"
"s_nop 1\n"
"v_mov_b32_dpp v2, v7 quad_perm:[1,1,3,3] row_mask:0xf bank_mask:0xf\n"
"s_mov_b64 exec, s[78:79]\n"
"v_cvt_pk_bf16_f32 v2, v7, v2\n"
"buffer_store_dword v2, v1, s[40:43], s75 offen offset:0 sc1\n"
"s_mov_b64 exec, -1\n"
"s_add_u32 s61, s61, 65536\n"
"s_add_u32 s60, s60, 65536\n"
"s_add_u32 s66, s54, 6\n"
"s_mov_b32 s78, 0x5555\n"
"s_cmp_eq_u32 s48, 3\n"
"s_cselect_b32 s79, 3, 0\n"
"s_cmp_eq_u32 s48, 0\n"
"s_cbranch_scc0 .Lr_142_%=\n"
"v_and_b32 v8, 15, v0\n"
"v_lshrrev_b32 v9, 4, v0\n"
"v_lshl_add_u32 v8, v8, 1, v9\n"
"v_lshlrev_b32 v8, 3, v8\n"
"v_lshlrev_b32 v8, 1, v8\n"
"ds_read_b128 v[40:43], v8 offset:2560\n"
"v_and_b32 v1, 3, v0\n"
"v_mul_u32_u24 v1, 0x5555, v1\n"
"v_bfe_i32 v3, v0, 2, 1\n"
"v_and_b32 v3, 0x3f80, v3\n"
"v_xor_b32 v2, 0x3f80, v3\n"
"v_mov_b64 v[4:5], v[2:3]\n"
"v_and_b32 v7, 15, v0\n"
"v_min_u32 v7, 9, v7\n"
"v_mul_u32_u24 v9, 160, v9\n"
"v_lshl_add_u32 v7, v7, 4, v9\n"
"s_add_u32 s73, s58, s59\n"
"s_add_u32 s73, s73, 131072\n"
"s_add_u32 s74, s73, 2560\n"
".Lr_142_%=:\n"
"s_cmp_eq_u32 s48, 0\n"
"s_cbranch_scc0 .Lr_143_%=\n"
"s_sleep 12\n"
"buffer_load_dwordx4 v[8:11], v7, s[40:43], s73 offen offset:0 sc1\n"
"buffer_load_dwordx4 v[12:15], v7, s[40:43], s73 offen offset:640 sc1\n"
"buffer_load_dwordx4 v[16:19], v7, s[40:43], s73 offen offset:1280 sc1\n"
"buffer_load_dwordx4 v[20:23], v7, s[40:43], s73 offen offset:1920 sc1\n"
"buffer_load_dwordx4 v[24:27], v7, s[40:43], s74 offen offset:0 sc1\n"
"buffer_load_dwordx4 v[28:31], v7, s[40:43], s74 offen offset:640 sc1\n"
"buffer_load_dwordx4 v[32:35], v7, s[40:43], s74 offen offset:1280 sc1\n"
"buffer_load_dwordx4 v[36:39], v7, s[40:43], s74 offen offset:1920 sc1\n"
".Lr_143_%=:\n"
"s_cmp_lg_u32 s48, 0\n"
"s_cbranch_scc0 .Lr_144_%=\n"
"v_and_b32 v2, 1, v0\n"
"v_mul_u32_u24 v2, 160, v2\n"
"v_lshrrev_b32 v3, 4, v0\n"
"v_lshl_add_u32 v1, v3, 4, v2\n"
"v_lshl_add_u32 v2, v3, 3, v2\n"
"s_waitcnt lgkmcnt(0)\n"
".Lr_144_%=:\n"
"s_cmp_eq_u32 s48, 0\n"
"s_cbranch_scc0 .Lr_145_%=\n"
".Lgb_146_%=:\n"
"s_waitcnt lgkmcnt(0)\n"
"s_waitcnt vmcnt(6)\n"
"v_smfmac_f32_16x16x64_bf16 v[40:43], v[2:5], v[8:15], v1\n"
"v_max_u32 v6, v8, v9\n"
"v_max3_u32 v6, v6, v10, v11\n"
"v_max3_u32 v6, v6, v12, v13\n"
"v_max3_u32 v6, v6, v14, v15\n"
"s_waitcnt vmcnt(4)\n"
"v_smfmac_f32_16x16x64_bf16 v[40:43], v[2:5], v[16:23], v1\n"
"v_max3_u32 v6, v6, v16, v17\n"
"v_max3_u32 v6, v6, v18, v19\n"
"v_max3_u32 v6, v6, v20, v21\n"
"v_max3_u32 v6, v6, v22, v23\n"
"v_and_b32 v8, 15, v0\n"
"v_lshrrev_b32 v9, 4, v0\n"
"v_lshl_add_u32 v8, v8, 1, v9\n"
"v_lshlrev_b32 v8, 3, v8\n"
"s_waitcnt vmcnt(2)\n"
"v_smfmac_f32_16x16x64_bf16 v[40:43], v[2:5], v[24:31], v1\n"
"v_max3_u32 v6, v6, v24, v25\n"
"v_max3_u32 v6, v6, v26, v27\n"
"v_max3_u32 v6, v6, v28, v29\n"
"v_max3_u32 v6, v6, v30, v31\n"
"s_waitcnt vmcnt(0)\n"
"v_smfmac_f32_16x16x64_bf16 v[40:43], v[2:5], v[32:39], v1\n"
"v_max3_u32 v6, v6, v32, v33\n"
"v_max3_u32 v6, v6, v34, v35\n"
"v_max3_u32 v6, v6, v36, v37\n"
"v_max3_u32 v6, v6, v38, v39\n"
"v_cmp_eq_u32 vcc, -1, v6\n"
"s_mov_b64 exec, 0x03ff03ff\n"
"s_nop 1\n"
"v_cvt_pk_bf16_f32 v10, v40, v41\n"
"v_cvt_pk_bf16_f32 v11, v42, v43\n"
"ds_write_b64 v8, v[10:11] offset:0\n"
"s_mov_b64 exec, -1\n"
"s_cbranch_vccnz .Lgr_147_%=\n"
".Lgo_148_%=:\n"
"v_and_b32 v2, 1, v0\n"
"v_mul_u32_u24 v2, 160, v2\n"
"v_lshrrev_b32 v3, 4, v0\n"
"v_lshl_add_u32 v1, v3, 4, v2\n"
"v_lshl_add_u32 v2, v3, 3, v2\n"
"s_waitcnt lgkmcnt(0)\n"
".Lr_145_%=:\n"
"s_barrier\n"
"ds_read_b128 v[18:21], v1 offset:0\n"
"ds_read_b128 v[22:25], v1 offset:64\n"
"ds_read_b64 v[26:27], v2 offset:128\n"
"s_cmp_eq_u32 s48, 0\n"
"s_cbranch_scc0 .Lr_149_%=\n"
"v_lshlrev_b32 v12, 1, v8\n"
"s_mov_b64 exec, 0x03ff03ff\n"
"ds_write_b128 v12, v[40:43] offset:2560\n"
"s_mov_b64 exec, -1\n"
".Lr_149_%=:\n"
"s_lshl2_add_u32 s70, s50, s48\n"
"s_lshl_b32 s70, s70, 7\n"
"v_lshl_add_u32 v16, v0, 3, s70\n"
"s_lshl_b32 s70, s50, 7\n"
"s_add_u32 s70, s70, 3584\n"
"s_mov_b32 exec_lo, 0\n"
"v_lshl_add_u32 v16, v0, 4, s70\n"
"s_mov_b32 exec_lo, -1\n"
"v_mov_b32 v11, s66\n"
"s_waitcnt lgkmcnt(2)\n"
"v_mfma_f32_16x16x32_bf16 v[28:31], v[18:21], v[98:101], 0\n"
"v_mfma_f32_16x16x32_bf16 v[32:35], v[18:21], v[108:111], 0\n"
"v_mfma_f32_16x16x32_bf16 v[36:39], v[18:21], v[118:121], 0\n"
"v_mfma_f32_16x16x32_bf16 v[40:43], v[18:21], v[18:21], 0\n"
"s_waitcnt lgkmcnt(1)\n"
"v_mfma_f32_16x16x32_bf16 v[28:31], v[22:25], v[102:105], v[28:31]\n"
"v_mfma_f32_16x16x32_bf16 v[32:35], v[22:25], v[112:115], v[32:35]\n"
"v_mfma_f32_16x16x32_bf16 v[36:39], v[22:25], v[122:125], v[36:39]\n"
"v_mfma_f32_16x16x32_bf16 v[40:43], v[22:25], v[22:25], v[40:43]\n"
"s_waitcnt lgkmcnt(0)\n"
"v_mfma_f32_16x16x16_bf16 v[28:31], v[26:27], v[106:107], v[28:31]\n"
"v_mfma_f32_16x16x16_bf16 v[32:35], v[26:27], v[116:117], v[32:35]\n"
"v_mfma_f32_16x16x16_bf16 v[36:39], v[26:27], v[126:127], v[36:39]\n"
"v_mfma_f32_16x16x16_bf16 v[40:43], v[26:27], v[26:27], v[40:43]\n"
"v_mov_b32_dpp v29, v28 quad_perm:[1,1,3,3] row_mask:0xf bank_mask:0xf\n"
"v_mov_b32_dpp v33, v32 quad_perm:[1,1,3,3] row_mask:0xf bank_mask:0xf\n"
"s_nop 1\n"
"v_mov_b32_dpp v37, v36 quad_perm:[1,1,3,3] row_mask:0xf bank_mask:0xf\n"
"v_cvt_pk_bf16_f32 v8, v28, v29\n"
"v_cvt_pk_bf16_f32 v9, v32, v33\n"
"v_cvt_pk_bf16_f32 v10, v36, v37\n"
"v_mov_b32_dpp v8, v40 quad_perm:[0,1,2,3] row_mask:0x4 bank_mask:0xf\n"
"s_mov_b64 exec, s[78:79]\n"
"buffer_store_dwordx4 v[8:11], v16, s[40:43], s56 offen offset:0 sc1\n"
"s_mov_b64 exec, -1\n"
"v_and_b32 v6, 1, v0\n"
"v_mul_u32_u24 v6, 0xaaaa, v6\n"
"v_add_u32 v6, 0x4444, v6\n"
"s_cmp_eq_u32 s48, 0\n"
"s_cbranch_scc0 .Lr_150_%=\n"
"v_and_b32 v38, 15, v0\n"
"v_lshlrev_b32 v38, 1, v38\n"
"s_mov_b32 s70, 0x44ee44\n"
"v_bfe_u32 v1, s70, v38, 2\n"
"s_mov_b32 s70, 0xf5a0a0\n"
"v_bfe_u32 v38, s70, v38, 2\n"
"v_mul_u32_u24 v1, 5, v1\n"
"v_lshlrev_b32 v2, 2, v38\n"
"v_lshlrev_b32 v1, v2, v1\n"
"v_lshlrev_b32 v38, v38, 1\n"
"v_bfe_i32 v2, v38, 0, 1\n"
"v_and_b32 v2, 0x3f80, v2\n"
"v_bfe_i32 v3, v38, 1, 1\n"
"v_and_b32 v3, 0x3f80, v3\n"
"v_bfe_i32 v4, v38, 2, 1\n"
"v_and_b32 v4, 0x3f80, v4\n"
"v_bfe_i32 v5, v38, 3, 1\n"
"v_and_b32 v5, 0x3f80, v5\n"
"v_and_b32 v7, 15, v0\n"
"v_bfe_u32 v38, v0, 4, 1\n"
"v_lshl_add_u32 v7, v7, 1, v38\n"
"v_and_b32 v38, 32, v0\n"
"v_add_lshl_u32 v7, v7, v38, 4\n"
"v_mov_b64 v[28:29], 0\n"
"v_mov_b64 v[30:31], 0\n"
"v_bfe_u32 v38, v0, 2, 2\n"
"v_lshrrev_b32 v36, 4, v0\n"
"v_mad_u32_u24 v38, v38, 3, v36\n"
"v_and_b32 v36, 3, v0\n"
"v_lshl_add_u32 v36, v38, 3, v36\n"
"v_lshlrev_b32 v36, 2, v36\n"
"v_lshrrev_b32 v42, 4, v0\n"
"v_and_b32 v38, 1, v0\n"
"v_lshl_add_u32 v42, v42, 1, v38\n"
"v_lshlrev_b32 v42, 4, v42\n"
"v_and_b32 v37, 14, v0\n"
"v_and_b32 v39, 1, v0\n"
"v_lshl_add_u32 v37, v37, 2, v39\n"
"v_lshlrev_b32 v37, 4, v37\n"
"v_add_u32 v37, 4096, v37\n"
"v_lshlrev_b32 v39, 4, v0\n"
"ds_read_b128 v[32:35], v39 offset:1024\n"
".Lr_150_%=:\n"
"s_cmp_eq_u32 s48, 0\n"
"s_cbranch_scc0 .Lr_151_%=\n"
"s_sleep 10\n"
"buffer_load_dwordx4 v[8:11], v7, s[40:43], s56 offen offset:0 sc1\n"
"buffer_load_dwordx4 v[12:15], v7, s[40:43], s56 offen offset:1024 sc1\n"
"buffer_load_dwordx4 v[16:19], v7, s[40:43], s56 offen offset:2048 sc1\n"
"buffer_load_dwordx4 v[20:23], v7, s[40:43], s56 offen offset:3072 sc1\n"
"buffer_load_dwordx4 v[24:27], v37, s[40:43], s56 offen offset:0 sc1\n"
".Lr_151_%=:\n"
"s_cmp_lg_u32 s48, 0\n"
"s_cbranch_scc0 .Lr_152_%=\n"
"v_lshrrev_b32 v42, 4, v0\n"
"v_and_b32 v38, 1, v0\n"
"v_lshl_add_u32 v42, v42, 1, v38\n"
"v_lshlrev_b32 v42, 4, v42\n"
"s_cmp_eq_u32 s48, 3\n"
"s_cbranch_scc0 .Lr_153_%=\n"
"v_lshlrev_b32 v7, 4, v0\n"
"ds_read_b128 v[98:101], v7 offset:4096\n"
"ds_read_b128 v[102:105], v7 offset:5120\n"
"ds_read_b128 v[106:109], v7 offset:6144\n"
"ds_read_b128 v[110:113], v7 offset:7168\n"
"ds_read_b128 v[114:117], v7 offset:8192\n"
"ds_read_b128 v[118:121], v7 offset:9216\n"
".Lr_153_%=:\n"
".Lr_152_%=:\n"
"s_cmp_eq_u32 s48, 0\n"
"s_cbranch_scc0 .Lr_154_%=\n"
".Lgb_155_%=:\n"
"s_waitcnt vmcnt(3)\n"
"v_smfmac_f32_16x16x64_bf16 v[28:31], v[2:5], v[8:15], v1\n"
"s_waitcnt vmcnt(1)\n"
"v_smfmac_f32_16x16x64_bf16 v[28:31], v[2:5], v[16:23], v1\n"
"s_waitcnt vmcnt(0)\n"
"v_add_f32_dpp v24, v24, v24 row_ror:2 row_mask:0xf bank_mask:0xf\n"
"v_bitop3_b32 v38, v11, v15, s66 bitop3:0x7e\n"
"v_bitop3_b32 v38, v19, v38, s66 bitop3:0xde\n"
"v_add_f32_dpp v24, v24, v24 row_ror:4 row_mask:0xf bank_mask:0xf\n"
"v_bitop3_b32 v38, v23, v38, s66 bitop3:0xde\n"
"v_bitop3_b32 v38, v27, v38, s66 bitop3:0xde\n"
"v_add_f32_dpp v24, v24, v24 row_ror:8 row_mask:0xf bank_mask:0xf\n"
"v_cmp_ne_u32 vcc, 0, v38\n"
"s_waitcnt lgkmcnt(0)\n"
"v_fmac_f32_dpp v28, v24, v32 quad_perm:[1,1,3,3] row_mask:0xf bank_mask:0xf\n"
"v_fmac_f32_dpp v29, v24, v33 quad_perm:[1,1,3,3] row_mask:0xf bank_mask:0xf\n"
"v_fmac_f32_dpp v30, v24, v34 quad_perm:[1,1,3,3] row_mask:0xf bank_mask:0xf\n"
"v_fmac_f32_dpp v31, v24, v35 quad_perm:[1,1,3,3] row_mask:0xf bank_mask:0xf\n"
"v_cvt_pk_bf16_f32 v40, v28, v29\n"
"v_cvt_pk_bf16_f32 v41, v30, v31\n"
"s_mov_b32 exec_hi, 0xffff\n"
"ds_write2_b32 v36, v40, v41 offset0:96 offset1:100\n"
"s_mov_b64 exec, 3\n"
"ds_write_b32 v39, v24 offset:320\n"
"s_mov_b64 exec, -1\n"
"s_cbranch_vccnz .Lgr_156_%=\n"
".Lgo_157_%=:\n"
".Lr_154_%=:\n"
"s_waitcnt lgkmcnt(0)\n"
"s_barrier\n"
"ds_read_b128 v[8:11], v42 offset:384\n"
"ds_read_b128 v[12:15], v42 offset:512\n"
"ds_read_b128 v[16:19], v42 offset:640\n"
"v_mov_b32 v5, 0\n"
"ds_read2_b32 v[20:21], v5 offset0:80 offset1:84\n"
"v_mov_b64 v[24:25], 0\n"
"v_mov_b64 v[28:29], 0\n"
"v_mov_b64 v[32:33], 0\n"
"v_mov_b64 v[36:37], 0\n"
"v_mov_b64 v[40:41], 0\n"
"s_lshl_b32 s70, s48, 2\n"
"v_and_or_b32 v1, v0, 48, s70\n"
"s_waitcnt lgkmcnt(3)\n"
"v_smfmac_f32_16x16x64_bf16 v[24:27], v[8:11], a[0:7], v6\n"
"s_waitcnt lgkmcnt(2)\n"
"v_smfmac_f32_16x16x64_bf16 v[24:27], v[12:15], a[8:15], v6\n"
"s_waitcnt lgkmcnt(1)\n"
"v_smfmac_f32_16x16x64_bf16 v[24:27], v[16:19], a[16:23], v6\n"
"s_waitcnt lgkmcnt(0)\n"
"v_smfmac_f32_16x16x64_bf16 v[28:31], v[8:11], a[24:31], v6\n"
"v_smfmac_f32_16x16x64_bf16 v[28:31], v[12:15], a[32:39], v6\n"
"v_smfmac_f32_16x16x64_bf16 v[28:31], v[16:19], a[40:47], v6\n"
"v_add_f32_dpp v22, v24, v25 quad_perm:[0,1,2,3] row_mask:0x1 bank_mask:0xf\n"
"v_mov_b64 v[24:25], 0\n"
"v_smfmac_f32_16x16x64_bf16 v[32:35], v[8:11], a[48:55], v6\n"
"v_smfmac_f32_16x16x64_bf16 v[32:35], v[12:15], a[56:63], v6\n"
"v_smfmac_f32_16x16x64_bf16 v[32:35], v[16:19], a[64:71], v6\n"
"v_add_f32_dpp v22, v28, v29 quad_perm:[0,1,2,3] row_mask:0x2 bank_mask:0xf\n"
"v_mov_b64 v[28:29], 0\n"
"v_smfmac_f32_16x16x64_bf16 v[36:39], v[8:11], a[72:79], v6\n"
"v_smfmac_f32_16x16x64_bf16 v[36:39], v[12:15], a[80:87], v6\n"
"v_smfmac_f32_16x16x64_bf16 v[36:39], v[16:19], a[88:95], v6\n"
"v_add_f32_dpp v22, v32, v33 quad_perm:[0,1,2,3] row_mask:0x4 bank_mask:0xf\n"
"v_mov_b64 v[32:33], 0\n"
"v_smfmac_f32_16x16x64_bf16 v[40:43], v[8:11], a[96:103], v6\n"
"v_smfmac_f32_16x16x64_bf16 v[40:43], v[12:15], a[104:111], v6\n"
"v_smfmac_f32_16x16x64_bf16 v[40:43], v[16:19], a[112:119], v6\n"
"v_add_f32_dpp v22, v36, v37 quad_perm:[0,1,2,3] row_mask:0x8 bank_mask:0xf\n"
"v_smfmac_f32_16x16x64_bf16 v[24:27], v[8:11], a[120:127], v6\n"
"v_smfmac_f32_16x16x64_bf16 v[24:27], v[12:15], a[128:135], v6\n"
"v_smfmac_f32_16x16x64_bf16 v[24:27], v[16:19], a[136:143], v6\n"
"v_add_f32_dpp v23, v40, v41 quad_perm:[0,1,2,3] row_mask:0x1 bank_mask:0xf\n"
"v_mul_f32 v21, 0x3acccccd, v21\n"
"v_smfmac_f32_16x16x64_bf16 v[28:31], v[8:11], a[144:151], v6\n"
"v_smfmac_f32_16x16x64_bf16 v[28:31], v[12:15], a[152:159], v6\n"
"v_smfmac_f32_16x16x64_bf16 v[28:31], v[16:19], a[160:167], v6\n"
"v_add_f32_dpp v23, v24, v25 quad_perm:[0,1,2,3] row_mask:0x2 bank_mask:0xf\n"
"v_mul_f32 v20, 0x3acccccd, v20\n"
"v_smfmac_f32_16x16x64_bf16 v[32:35], v[8:11], a[168:175], v6\n"
"v_smfmac_f32_16x16x64_bf16 v[32:35], v[12:15], a[176:183], v6\n"
"v_smfmac_f32_16x16x64_bf16 v[32:35], v[16:19], a[184:191], v6\n"
"v_add_f32_dpp v23, v28, v29 quad_perm:[0,1,2,3] row_mask:0x4 bank_mask:0xf\n"
"v_fma_f32 v20, -v21, v21, v20\n"
"v_max_f32 v20, 0, v20\n"
"v_add_f32 v20, 0x3727c5ac, v20\n"
"s_mov_b32 s70, 0x3f803f80\n"
"s_nop 2\n"
"v_add_f32_dpp v23, v32, v33 quad_perm:[0,1,2,3] row_mask:0x8 bank_mask:0xf\n"
"v_max_f32 v2, v22, v23\n"
"v_rsq_f32 v20, v20\n"
"s_nop 0\n"
"v_max_f32_dpp v2, v2, v2 row_ror:1 row_mask:0xf bank_mask:0xf\n"
"v_mul_f32 v20, 0x3dd53b95, v20\n"
"s_nop 0\n"
"v_max_f32_dpp v2, v2, v2 row_ror:2 row_mask:0xf bank_mask:0xf\n"
"v_mov_b64 v[8:9], 0\n"
"v_mov_b64 v[12:13], 0\n"
"v_max_f32_dpp v2, v2, v2 row_ror:4 row_mask:0xf bank_mask:0xf\n"
"v_mov_b64 v[16:17], 0\n"
"v_mov_b64 v[28:29], 0\n"
"v_max_f32_dpp v2, v2, v2 row_ror:8 row_mask:0xf bank_mask:0xf\n"
"s_nop 1\n"
"v_max_f32_dpp v2, v2, v2 row_bcast:15 row_mask:0xa bank_mask:0xf\n"
"s_nop 1\n"
"v_max_f32_dpp v2, v2, v2 row_bcast:31 row_mask:0xc bank_mask:0xf\n"
"v_mul_f32 v2, v2, v20\n"
"s_nop 0\n"
"v_readlane_b32 s1, v2, 63\n"
"ds_write_b32 v1, v2 offset:3904\n"
"s_nop 0\n"
"v_fma_f32 v22, v22, v20, -s1\n"
"v_fma_f32 v23, v23, v20, -s1\n"
"s_nop 0\n"
"v_exp_f32_dpp v36, v22 quad_perm:[0,2,0,2] row_mask:0xf bank_mask:0xf\n"
"v_exp_f32_dpp v37, v22 quad_perm:[1,3,1,3] row_mask:0xf bank_mask:0xf\n"
"v_exp_f32_dpp v40, v23 quad_perm:[0,2,0,2] row_mask:0xf bank_mask:0xf\n"
"v_exp_f32_dpp v41, v23 quad_perm:[1,3,1,3] row_mask:0xf bank_mask:0xf\n"
"v_cvt_pk_bf16_f32 v36, v36, v37\n"
"v_cvt_pk_bf16_f32 v40, v40, v41\n"
"v_dot2_f32_bf16 v3, v36, s70, 0\n"
"v_mov_b32_dpp v37, v36 row_ror:4 row_mask:0xf bank_mask:0xf\n"
"v_mov_b32_dpp v38, v36 row_ror:8 row_mask:0xf bank_mask:0xf\n"
"v_mov_b32_dpp v39, v36 row_ror:12 row_mask:0xf bank_mask:0xf\n"
"v_dot2_f32_bf16 v3, v40, s70, v3\n"
"v_mov_b32_dpp v41, v40 row_ror:4 row_mask:0xf bank_mask:0xf\n"
"v_mov_b32_dpp v42, v40 row_ror:8 row_mask:0xf bank_mask:0xf\n"
"v_mov_b32_dpp v43, v40 row_ror:12 row_mask:0xf bank_mask:0xf\n"
"v_smfmac_f32_16x16x64_bf16 v[8:11], v[36:39], a[192:199], v6\n"
"s_nop 0\n"
"v_smfmac_f32_16x16x64_bf16 v[8:11], v[40:43], v[160:167], v6\n"
"s_waitcnt lgkmcnt(0)\n"
"s_barrier\n"
"ds_read_b128 v[24:27], v5 offset:3952\n"
"v_smfmac_f32_16x16x64_bf16 v[12:15], v[36:39], a[200:207], v6\n"
"v_smfmac_f32_16x16x64_bf16 v[12:15], v[40:43], v[168:175], v6\n"
"v_add_f32_dpp v20, v8, v9 quad_perm:[0,1,2,3] row_mask:0x1 bank_mask:0xf\n"
"v_mov_b64 v[8:9], 0\n"
"s_add_u32 s66, s54, 7\n"
"s_lshl_b32 s72, s50, 7\n"
"s_add_u32 s72, s72, s56\n"
"s_add_u32 s72, s72, 6144\n"
"v_smfmac_f32_16x16x64_bf16 v[16:19], v[36:39], a[208:215], v6\n"
"v_smfmac_f32_16x16x64_bf16 v[16:19], v[40:43], v[176:183], v6\n"
"v_add_f32_dpp v20, v12, v13 quad_perm:[0,1,2,3] row_mask:0x2 bank_mask:0xf\n"
"v_mov_b64 v[12:13], 0\n"
"v_smfmac_f32_16x16x64_bf16 v[28:31], v[36:39], a[216:223], v6\n"
"v_smfmac_f32_16x16x64_bf16 v[28:31], v[40:43], v[184:191], v6\n"
"v_add_f32_dpp v20, v16, v17 quad_perm:[0,1,2,3] row_mask:0x4 bank_mask:0xf\n"
"v_mov_b64 v[16:17], 0\n"
"s_waitcnt lgkmcnt(0)\n"
"v_max3_f32 v2, v24, v25, v26\n"
"v_max_f32 v2, v2, v27\n"
"v_smfmac_f32_16x16x64_bf16 v[8:11], v[36:39], a[224:231], v6\n"
"v_smfmac_f32_16x16x64_bf16 v[8:11], v[40:43], v[192:199], v6\n"
"v_add_f32_dpp v20, v28, v29 quad_perm:[0,1,2,3] row_mask:0x8 bank_mask:0xf\n"
"v_mov_b64 v[28:29], 0\n"
"s_cmp_eq_u32 s48, 1\n"
"s_cbranch_scc0 .Lr_158_%=\n"
"v_mov_b32 v24, v2\n"
"v_mov_b32 v25, s66\n"
"s_mov_b64 exec, 1\n"
"buffer_store_dwordx2 v[24:25], off, s[40:43], s72 sc1\n"
"s_mov_b64 exec, -1\n"
".Lr_158_%=:\n"
"v_smfmac_f32_16x16x64_bf16 v[12:15], v[36:39], a[232:239], v6\n"
"v_smfmac_f32_16x16x64_bf16 v[12:15], v[40:43], v[200:207], v6\n"
"v_add_f32_dpp v21, v8, v9 quad_perm:[0,1,2,3] row_mask:0x1 bank_mask:0xf\n"
"v_mov_b64 v[8:9], 0\n"
"v_sub_f32 v4, s1, v2\n"
"v_exp_f32 v4, v4\n"
"v_smfmac_f32_16x16x64_bf16 v[16:19], v[36:39], a[240:247], v6\n"
"v_smfmac_f32_16x16x64_bf16 v[16:19], v[40:43], v[208:215], v6\n"
"v_add_f32_dpp v21, v12, v13 quad_perm:[0,1,2,3] row_mask:0x2 bank_mask:0xf\n"
"v_mov_b64 v[12:13], 0\n"
"v_smfmac_f32_16x16x64_bf16 v[28:31], v[36:39], a[248:255], v6\n"
"v_smfmac_f32_16x16x64_bf16 v[28:31], v[40:43], v[216:223], v6\n"
"v_add_f32_dpp v21, v16, v17 quad_perm:[0,1,2,3] row_mask:0x4 bank_mask:0xf\n"
"v_mov_b64 v[16:17], 0\n"
"v_smfmac_f32_16x16x64_bf16 v[8:11], v[36:39], v[128:135], v6\n"
"v_smfmac_f32_16x16x64_bf16 v[8:11], v[40:43], v[224:231], v6\n"
"v_add_f32_dpp v21, v28, v29 quad_perm:[0,1,2,3] row_mask:0x8 bank_mask:0xf\n"
"v_mov_b64 v[28:29], 0\n"
"v_smfmac_f32_16x16x64_bf16 v[12:15], v[36:39], v[136:143], v6\n"
"v_smfmac_f32_16x16x64_bf16 v[12:15], v[40:43], v[232:239], v6\n"
"v_add_f32_dpp v22, v8, v9 quad_perm:[0,1,2,3] row_mask:0x1 bank_mask:0xf\n"
"v_smfmac_f32_16x16x64_bf16 v[16:19], v[36:39], v[144:151], v6\n"
"v_smfmac_f32_16x16x64_bf16 v[16:19], v[40:43], v[240:247], v6\n"
"v_add_f32_dpp v22, v12, v13 quad_perm:[0,1,2,3] row_mask:0x2 bank_mask:0xf\n"
"v_smfmac_f32_16x16x64_bf16 v[28:31], v[36:39], v[152:159], v6\n"
"v_smfmac_f32_16x16x64_bf16 v[28:31], v[40:43], v[248:255], v6\n"
"v_add_f32_dpp v22, v16, v17 quad_perm:[0,1,2,3] row_mask:0x4 bank_mask:0xf\n"
"s_mul_i32 s70, s48, 768\n"
"v_lshl_add_u32 v33, v0, 2, s70\n"
"v_mul_f32 v20, v20, v4\n"
"ds_write_b32 v33, v20 offset:4096\n"
"v_mul_f32 v21, v21, v4\n"
"ds_write_b32 v33, v21 offset:4352\n"
"s_nop 0\n"
"v_add_f32_dpp v22, v28, v29 quad_perm:[0,1,2,3] row_mask:0x8 bank_mask:0xf\n"
"v_mul_f32 v22, v22, v4\n"
"ds_write_b32 v33, v22 offset:4608\n"
"v_mul_f32 v3, v3, v4\n"
"ds_write_b32 v33, v3 offset:7168\n"
"v_mul_u32_u24 v32, 24, v0\n"
"v_add_u32 v32, 4096, v32\n"
"v_add_u32 v33, 1536, v32\n"
"v_lshlrev_b32 v1, 4, v0\n"
"s_waitcnt lgkmcnt(0)\n"
"s_barrier\n"
"s_cmp_eq_u32 s48, 0\n"
"s_cbranch_scc0 .Lr_159_%=\n"
"ds_read2_b64 v[8:11], v32 offset0:0 offset1:1\n"
"ds_read2_b64 v[12:15], v32 offset0:2 offset1:96\n"
"ds_read2_b64 v[16:19], v32 offset0:97 offset1:98\n"
"ds_read2_b64 v[20:23], v33 offset0:0 offset1:1\n"
"ds_read2_b64 v[24:27], v33 offset0:2 offset1:96\n"
"ds_read2_b64 v[28:31], v33 offset0:97 offset1:98\n"
"s_lshl_b32 s71, s50, 9\n"
"s_add_u32 s71, s71, s57\n"
"v_mov_b32 v35, s66\n"
"s_mov_b32 exec_hi, 0\n"
"s_waitcnt lgkmcnt(3)\n"
"v_pk_add_f32 v[8:9], v[8:9], v[14:15]\n"
"v_pk_add_f32 v[10:11], v[10:11], v[16:17]\n"
"v_pk_add_f32 v[12:13], v[12:13], v[18:19]\n"
"s_waitcnt lgkmcnt(0)\n"
"v_pk_add_f32 v[20:21], v[20:21], v[26:27]\n"
"v_pk_add_f32 v[22:23], v[22:23], v[28:29]\n"
"v_pk_add_f32 v[24:25], v[24:25], v[30:31]\n"
"v_pk_add_f32 v[8:9], v[8:9], v[20:21]\n"
"v_pk_add_f32 v[10:11], v[10:11], v[22:23]\n"
"v_pk_add_f32 v[12:13], v[12:13], v[24:25]\n"
"v_cvt_pk_bf16_f32 v32, v8, v9\n"
"v_cvt_pk_bf16_f32 v33, v10, v11\n"
"v_cvt_pk_bf16_f32 v34, v12, v13\n"
"buffer_store_dwordx4 v[32:35], v1, s[40:43], s71 offen offset:0 sc1\n"
"s_mov_b32 exec_hi, -1\n"
".Lr_159_%=:\n"
"s_cmp_eq_u32 s48, 1\n"
"s_cbranch_scc0 .Lr_160_%=\n"
"v_lshlrev_b32 v5, 2, v0\n"
"ds_read2st64_b32 v[8:9], v5 offset0:28 offset1:31\n"
"ds_read2st64_b32 v[10:11], v5 offset0:34 offset1:37\n"
"v_mov_b32 v13, s66\n"
"s_waitcnt lgkmcnt(0)\n"
"v_pk_add_f32 v[8:9], v[8:9], v[10:11]\n"
"v_add_f32 v12, v8, v9\n"
"s_nop 1\n"
"v_add_f32_dpp v12, v12, v12 row_ror:1 row_mask:0xf bank_mask:0xf\n"
"s_nop 1\n"
"v_add_f32_dpp v12, v12, v12 row_ror:4 row_mask:0xf bank_mask:0xf\n"
"s_nop 1\n"
"v_add_f32_dpp v12, v12, v12 row_ror:8 row_mask:0xf bank_mask:0xf\n"
"s_nop 1\n"
"v_add_f32_dpp v12, v12, v12 row_bcast:15 row_mask:0xa bank_mask:0xf\n"
"s_nop 1\n"
"v_add_f32_dpp v12, v12, v12 row_bcast:31 row_mask:0xc bank_mask:0xf\n"
"s_mov_b32 exec_lo, 0\n"
"s_brev_b32 exec_hi, 1\n"
"buffer_store_dwordx2 v[12:13], off, s[40:43], s72 offset:1024 sc1\n"
"s_mov_b64 exec, -1\n"
".Lr_160_%=:\n"
"s_mul_i32 s75, s49, 160\n"
"s_add_u32 s75, s75, s58\n"
"s_lshl_b32 s78, s48, 5\n"
"s_add_u32 s75, s75, s78\n"
"s_add_u32 s75, s75, 262144\n"
"s_mov_b32 s78, 0x55555555\n"
"s_cmp_eq_u32 s48, 3\n"
"s_cselect_b32 s79, s78, 0\n"
"s_cmp_eq_u32 s48, 0\n"
"s_cbranch_scc0 .Lr_161_%=\n"
"v_and_b32 v31, 48, v0\n"
"v_lshlrev_b32 v31, 3, v31\n"
"v_add_u32 v6, 6144, v31\n"
"buffer_load_dwordx2 v[24:25], v6, s[40:43], s56 offen offset:0 sc1\n"
"buffer_load_dwordx2 v[28:29], v6, s[40:43], s56 offen offset:512 sc1\n"
"v_and_b32 v36, 15, v0\n"
"v_lshlrev_b32 v36, 1, v36\n"
"s_mov_b32 s70, 0x4e44e4\n"
"v_bfe_u32 v1, s70, v36, 2\n"
"s_mov_b32 s70, 0xfaa500\n"
"v_bfe_u32 v36, s70, v36, 2\n"
"v_mul_u32_u24 v1, 5, v1\n"
"v_lshlrev_b32 v2, 2, v36\n"
"v_lshlrev_b32 v1, v2, v1\n"
"v_lshlrev_b32 v36, v36, 1\n"
"v_bfe_i32 v2, v36, 0, 1\n"
"v_bfe_i32 v3, v36, 1, 1\n"
"v_bfe_i32 v4, v36, 2, 1\n"
"v_bfe_i32 v5, v36, 3, 1\n"
"v_and_b32 v7, 15, v0\n"
"v_bfe_u32 v36, v0, 4, 1\n"
"v_lshl_add_u32 v7, v7, 1, v36\n"
"v_and_b32 v36, 32, v0\n"
"v_add_lshl_u32 v7, v7, v36, 4\n"
"v_mov_b64 v[32:33], 0\n"
"v_mov_b64 v[34:35], 0\n"
".Lr_161_%=:\n"
"v_lshrrev_b32 v229, 2, v0\n"
"v_and_b32 v230, 12, v229\n"
"v_add_u32 v229, v229, v230\n"
"v_lshrrev_b32 v230, 1, v229\n"
"v_and_b32 v230, 4, v230\n"
"v_and_or_b32 v230, v229, 2, v230\n"
"v_lshlrev_b32 v230, 1, v230\n"
"v_and_or_b32 v230, v0, 3, v230\n"
"v_lshlrev_b32 v230, 3, v230\n"
"v_mul_u32_u24 v229, 384, v229\n"
"v_add_u32 v228, v229, v230\n"
"s_mul_i32 s70, s48, 3\n"
"s_add_u32 s70, s70, 0\n"
"s_and_b32 s71, s70, 3\n"
"s_lshl_b32 s71, s71, 5\n"
"s_lshr_b32 s70, s70, 2\n"
"s_lshl_b32 s70, s70, 7\n"
"s_add_u32 s70, s70, 47104\n"
"v_xor_b32 v229, s71, v228\n"
"v_add_u32 v229, s70, v229\n"
"s_mul_i32 s70, s48, 3\n"
"s_add_u32 s70, s70, 1\n"
"s_and_b32 s71, s70, 3\n"
"s_lshl_b32 s71, s71, 5\n"
"s_lshr_b32 s70, s70, 2\n"
"s_lshl_b32 s70, s70, 7\n"
"s_add_u32 s70, s70, 47104\n"
"v_xor_b32 v230, s71, v228\n"
"v_add_u32 v230, s70, v230\n"
"s_mul_i32 s70, s48, 3\n"
"s_add_u32 s70, s70, 2\n"
"s_and_b32 s71, s70, 3\n"
"s_lshl_b32 s71, s71, 5\n"
"s_lshr_b32 s70, s70, 2\n"
"s_lshl_b32 s70, s70, 7\n"
"s_add_u32 s70, s70, 47104\n"
"v_xor_b32 v231, s71, v228\n"
"v_add_u32 v231, s70, v231\n"
"ds_read_b64_tr_b16 a[144:145], v229 offset:0\n"
"ds_read_b64_tr_b16 a[146:147], v229 offset:1536\n"
"ds_read_b64_tr_b16 a[148:149], v229 offset:12288\n"
"ds_read_b64_tr_b16 a[150:151], v229 offset:13824\n"
"ds_read_b64_tr_b16 a[152:153], v229 offset:24576\n"
"ds_read_b64_tr_b16 a[154:155], v229 offset:26112\n"
"ds_read_b64_tr_b16 a[156:157], v230 offset:0\n"
"ds_read_b64_tr_b16 a[158:159], v230 offset:1536\n"
"ds_read_b64_tr_b16 a[160:161], v230 offset:12288\n"
"ds_read_b64_tr_b16 a[162:163], v230 offset:13824\n"
"ds_read_b64_tr_b16 a[164:165], v230 offset:24576\n"
"ds_read_b64_tr_b16 a[166:167], v230 offset:26112\n"
"ds_read_b64_tr_b16 a[168:169], v231 offset:0\n"
"ds_read_b64_tr_b16 a[170:171], v231 offset:1536\n"
"ds_read_b64_tr_b16 a[172:173], v231 offset:12288\n"
"ds_read_b64_tr_b16 a[174:175], v231 offset:13824\n"
"ds_read_b64_tr_b16 a[176:177], v231 offset:24576\n"
"ds_read_b64_tr_b16 a[178:179], v231 offset:26112\n"
"v_lshrrev_b32 v229, 2, v0\n"
"v_and_b32 v228, 12, v229\n"
"v_add_u32 v229, v229, v228\n"
"v_mul_u32_u24 v229, 20, v229\n"
"v_and_b32 v228, 3, v0\n"
"v_add_lshl_u32 v228, v228, v229, 3\n"
"s_lshl_b32 s70, s48, 5\n"
"s_add_u32 s70, s70, 83968\n"
"v_add_u32 v228, s70, v228\n"
"ds_read_b64_tr_b16 a[180:181], v228 offset:0\n"
"ds_read_b64_tr_b16 a[182:183], v228 offset:640\n"
"ds_read_b64_tr_b16 a[184:185], v228 offset:5120\n"
"ds_read_b64_tr_b16 a[186:187], v228 offset:5760\n"
"ds_read_b64_tr_b16 a[188:189], v228 offset:10240\n"
"ds_read_b64_tr_b16 a[190:191], v228 offset:10880\n"
"ds_read_b64_tr_b16 a[192:193], v228 offset:15360\n"
"ds_read_b64_tr_b16 a[194:195], v228 offset:16000\n"
"ds_read_b64_tr_b16 a[196:197], v228 offset:20480\n"
"ds_read_b64_tr_b16 a[198:199], v228 offset:21120\n"
"ds_read_b64_tr_b16 a[200:201], v228 offset:25600\n"
"ds_read_b64_tr_b16 a[202:203], v228 offset:26240\n"
"s_cmp_eq_u32 s48, 3\n"
"s_cbranch_scc0 .Lr_162_%=\n"
"ds_read_b64_tr_b16 a[204:205], v228 offset:32\n"
"ds_read_b64_tr_b16 a[206:207], v228 offset:672\n"
"ds_read_b64_tr_b16 a[208:209], v228 offset:5152\n"
"ds_read_b64_tr_b16 a[210:211], v228 offset:5792\n"
"ds_read_b64_tr_b16 a[212:213], v228 offset:10272\n"
"ds_read_b64_tr_b16 a[214:215], v228 offset:10912\n"
"ds_read_b64_tr_b16 a[216:217], v228 offset:15392\n"
"ds_read_b64_tr_b16 a[218:219], v228 offset:16032\n"
"ds_read_b64_tr_b16 a[220:221], v228 offset:20512\n"
"ds_read_b64_tr_b16 a[222:223], v228 offset:21152\n"
"ds_read_b64_tr_b16 a[224:225], v228 offset:25632\n"
"ds_read_b64_tr_b16 a[226:227], v228 offset:26272\n"
".Lr_162_%=:\n"
"s_cmp_lg_u32 s48, 0\n"
"s_cbranch_scc0 .Lr_163_%=\n"
"s_mul_i32 s70, s48, 12288\n"
"s_add_u32 s70, s70, -2048\n"
"v_and_b32 v7, 15, v0\n"
"v_lshrrev_b32 v40, 1, v7\n"
"v_lshrrev_b32 v41, 4, v0\n"
"v_xor_b32 v40, v40, v41\n"
"v_mul_u32_u24 v7, 24, v7\n"
"v_add_lshl_u32 v40, v40, v7, 4\n"
"v_add_u32 v40, s70, v40\n"
"v_xor_b32 v41, 64, v40\n"
"ds_read_b128 a[96:99], v40 offset:0\n"
"ds_read_b128 a[100:103], v41 offset:0\n"
"ds_read_b128 a[104:107], v40 offset:128\n"
"ds_read_b128 a[108:111], v41 offset:128\n"
"ds_read_b128 a[112:115], v40 offset:256\n"
"ds_read_b128 a[116:119], v41 offset:256\n"
"ds_read_b128 a[120:123], v40 offset:6144\n"
"ds_read_b128 a[124:127], v41 offset:6144\n"
"ds_read_b128 a[128:131], v40 offset:6272\n"
"ds_read_b128 a[132:135], v41 offset:6272\n"
"ds_read_b128 a[136:139], v40 offset:6400\n"
"ds_read_b128 a[140:143], v41 offset:6400\n"
".Lr_163_%=:\n"
"s_cmp_eq_u32 s48, 0\n"
"s_cbranch_scc0 .Lr_164_%=\n"
"s_sleep 3\n"
"buffer_load_dwordx4 v[8:11], v7, s[40:43], s57 offen offset:0 sc1\n"
"buffer_load_dwordx4 v[12:15], v7, s[40:43], s57 offen offset:1024 sc1\n"
"buffer_load_dwordx4 v[16:19], v7, s[40:43], s57 offen offset:2048 sc1\n"
"buffer_load_dwordx4 v[20:23], v7, s[40:43], s57 offen offset:3072 sc1\n"
"buffer_load_dwordx2 v[26:27], v6, s[40:43], s56 offen offset:1024 sc1\n"
"buffer_load_dwordx2 v[30:31], v6, s[40:43], s56 offen offset:1536 sc1\n"
".Lr_164_%=:\n"
"s_cmp_lg_u32 s48, 0\n"
"s_cbranch_scc0 .Lr_165_%=\n"
"v_lshrrev_b32 v42, 4, v0\n"
"v_and_b32 v38, 1, v0\n"
"v_lshl_add_u32 v42, v42, 1, v38\n"
"v_lshlrev_b32 v42, 4, v42\n"
".Lr_165_%=:\n"
"s_cmp_eq_u32 s48, 0\n"
"s_cbranch_scc0 .Lr_166_%=\n"
".Lgb_167_%=:\n"
"s_waitcnt vmcnt(6)\n"
"v_max_f32 v36, v24, v28\n"
"s_nop 1\n"
"v_max_f32_dpp v36, v36, v36 row_bcast:15 row_mask:0xa bank_mask:0xf\n"
"s_nop 1\n"
"v_max_f32_dpp v36, v36, v36 row_bcast:31 row_mask:0xc bank_mask:0xf\n"
"s_nop 0\n"
"v_readlane_b32 s70, v36, 63\n"
"v_bitop3_b32 v29, v25, v29, s66 bitop3:0x7e\n"
"s_nop 0\n"
"v_subrev_f32 v24, s70, v24\n"
"v_subrev_f32 v28, s70, v28\n"
"v_exp_f32 v24, v24\n"
"v_exp_f32 v28, v28\n"
"v_cvt_pk_bf16_f32 v24, v24, 0\n"
"v_cvt_pk_bf16_f32 v28, v28, 0\n"
"v_and_b32 v36, 15, v0\n"
"v_lshrrev_b32 v25, 4, v0\n"
"v_mad_u32_u24 v36, v36, 3, v25\n"
"v_and_b32 v25, 60, v36\n"
"v_add_lshl_u32 v25, v25, v36, 2\n"
"v_and_b32 v36, v2, v24\n"
"v_and_b32 v37, v3, v24\n"
"v_and_b32 v38, v4, v24\n"
"v_and_b32 v39, v5, v24\n"
"v_and_b32 v40, v2, v28\n"
"v_and_b32 v41, v3, v28\n"
"v_and_b32 v42, v4, v28\n"
"v_and_b32 v43, v5, v28\n"
"v_lshlrev_b32 v24, 16, v24\n"
"v_lshlrev_b32 v28, 16, v28\n"
"s_waitcnt vmcnt(4)\n"
"v_smfmac_f32_16x16x64_bf16 v[32:35], v[36:39], v[8:15], v1\n"
"s_waitcnt vmcnt(2)\n"
"v_smfmac_f32_16x16x64_bf16 v[32:35], v[40:43], v[16:23], v1\n"
"s_waitcnt vmcnt(0)\n"
"v_mul_f32 v24, v24, v26\n"
"v_fmac_f32 v24, v28, v30\n"
"v_bitop3_b32 v29, v11, v29, s66 bitop3:0xde\n"
"v_bitop3_b32 v29, v15, v29, s66 bitop3:0xde\n"
"v_add_f32_dpp v24, v24, v24 row_bcast:15 row_mask:0xa bank_mask:0xf\n"
"v_bitop3_b32 v29, v19, v29, s66 bitop3:0xde\n"
"v_bitop3_b32 v29, v23, v29, s66 bitop3:0xde\n"
"v_add_f32_dpp v24, v24, v24 row_bcast:31 row_mask:0xc bank_mask:0xf\n"
"v_bitop3_b32 v29, v27, v29, s66 bitop3:0xde\n"
"v_rcp_f32 v24, v24\n"
"v_bitop3_b32 v29, v31, v29, s66 bitop3:0xde\n"
"v_readlane_b32 s70, v24, 63\n"
"v_cmp_ne_u32 vcc, 0, v29\n"
"s_mov_b32 exec_hi, 0xffff\n"
"v_mul_f32 v32, s70, v32\n"
"v_mul_f32 v33, s70, v33\n"
"v_mul_f32 v34, s70, v34\n"
"v_mul_f32 v35, s70, v35\n"
"v_cvt_pk_bf16_f32 v36, v32, v33\n"
"v_cvt_pk_bf16_f32 v37, v34, v35\n"
"ds_write2_b32 v25, v36, v37 offset0:96 offset1:100\n"
"s_mov_b32 exec_hi, -1\n"
"s_cbranch_vccnz .Lgr_168_%=\n"
".Lgo_169_%=:\n"
"v_lshrrev_b32 v42, 4, v0\n"
"v_and_b32 v36, 1, v0\n"
"v_lshl_add_u32 v42, v42, 1, v36\n"
"v_lshlrev_b32 v42, 4, v42\n"
".Lr_166_%=:\n"
"s_waitcnt lgkmcnt(0)\n"
"s_barrier\n"
"ds_read_b128 v[32:35], v42 offset:384\n"
"ds_read_b128 v[36:39], v42 offset:512\n"
"ds_read_b128 v[40:43], v42 offset:640\n"
"v_and_b32 v6, 1, v0\n"
"v_mul_u32_u24 v6, 0xaaaa, v6\n"
"v_add_u32 v6, 0x4444, v6\n"
"v_mov_b64 v[8:9], 0\n"
"v_mov_b64 v[2:3], 0\n"
"v_and_b32 v1, 15, v0\n"
"v_and_b32 v4, 32, v0\n"
"v_lshl_add_u32 v1, v1, 1, v4\n"
"v_and_b32 v4, 16, v0\n"
"v_lshl_add_u32 v1, v4, 9, v1\n"
"s_waitcnt lgkmcnt(2)\n"
"v_smfmac_f32_16x16x64_bf16 v[2:5], v[32:35], v[44:51], v6\n"
"v_smfmac_f32_16x16x64_bf16 v[8:11], v[32:35], v[98:105], v6\n"
"s_waitcnt lgkmcnt(1)\n"
"v_smfmac_f32_16x16x64_bf16 v[2:5], v[36:39], v[52:59], v6\n"
"v_smfmac_f32_16x16x64_bf16 v[8:11], v[36:39], v[106:113], v6\n"
"s_waitcnt lgkmcnt(0)\n"
"v_smfmac_f32_16x16x64_bf16 v[2:5], v[40:43], v[60:67], v6\n"
"v_smfmac_f32_16x16x64_bf16 v[8:11], v[40:43], v[114:121], v6\n"
"s_nop 3\n"
"v_add_f32 v12, v2, v3\n"
"s_nop 2\n"
"v_add_f32_dpp v12, v8, v9 quad_perm:[0,1,2,3] row_mask:0xc bank_mask:0xf\n"
"s_nop 1\n"
"v_mov_b32_dpp v2, v12 quad_perm:[1,1,3,3] row_mask:0xf bank_mask:0xf\n"
"s_mov_b64 exec, s[78:79]\n"
"v_cvt_pk_bf16_f32 v2, v12, v2\n"
"buffer_store_dword v2, v1, s[40:43], s75 offen offset:0 sc1\n"
"s_mov_b64 exec, -1\n"
"s_barrier\n"
"s_cmp_lg_u32 s48, 0\n"
"s_cbranch_scc0 .Lr_170_%=\n"
"v_lshl_add_u32 v1, s48, 5, v0\n"
"v_subrev_u32 v1, 32, v1\n"
"v_lshrrev_b32 v1, 6, v1\n"
"v_xor_b32 v1, v0, v1\n"
"v_lshlrev_b32 v1, 4, v1\n"
"s_lshl_b32 s70, s48, 10\n"
"s_add_u32 s70, s70, -1024\n"
"s_add_u32 m0, s70, 65536\n"
"v_xor_b32 v2, 0, v1\n"
"buffer_load_dwordx4 v2, s[28:31], s70 offen offset:0 lds\n"
"s_add_u32 s70, s70, 3072\n"
"s_add_u32 m0, s70, 65536\n"
"v_xor_b32 v2, 32, v1\n"
"buffer_load_dwordx4 v2, s[28:31], s70 offen offset:0 lds\n"
"s_add_u32 s70, s70, 3072\n"
"s_add_u32 m0, s70, 65536\n"
"v_xor_b32 v2, 64, v1\n"
"buffer_load_dwordx4 v2, s[28:31], s70 offen offset:0 lds\n"
"s_add_u32 s70, s70, 3072\n"
"s_add_u32 m0, s70, 65536\n"
"v_xor_b32 v2, 96, v1\n"
"buffer_load_dwordx4 v2, s[28:31], s70 offen offset:0 lds\n"
"s_add_u32 s70, s70, 3072\n"
"s_add_u32 m0, s70, 65536\n"
"v_xor_b32 v2, 128, v1\n"
"buffer_load_dwordx4 v2, s[28:31], s70 offen offset:0 lds\n"
"s_add_u32 s70, s70, 3072\n"
"s_add_u32 m0, s70, 65536\n"
"v_xor_b32 v2, 160, v1\n"
"buffer_load_dwordx4 v2, s[28:31], s70 offen offset:0 lds\n"
"s_add_u32 s70, s70, 3072\n"
"s_add_u32 m0, s70, 65536\n"
"v_xor_b32 v2, 192, v1\n"
"buffer_load_dwordx4 v2, s[28:31], s70 offen offset:0 lds\n"
"s_add_u32 s70, s70, 3072\n"
"s_add_u32 m0, s70, 65536\n"
"v_xor_b32 v2, 224, v1\n"
"buffer_load_dwordx4 v2, s[28:31], s70 offen offset:0 lds\n"
"s_add_u32 s70, s70, 3072\n"
"s_add_u32 m0, s70, 65536\n"
"v_xor_b32 v2, 0, v1\n"
"buffer_load_dwordx4 v2, s[28:31], s70 offen offset:0 lds\n"
"s_add_u32 s70, s70, 3072\n"
"s_add_u32 m0, s70, 65536\n"
"v_xor_b32 v2, 32, v1\n"
"buffer_load_dwordx4 v2, s[28:31], s70 offen offset:0 lds\n"
"s_add_u32 s70, s70, 3072\n"
"s_add_u32 m0, s70, 65536\n"
"v_xor_b32 v2, 64, v1\n"
"buffer_load_dwordx4 v2, s[28:31], s70 offen offset:0 lds\n"
"s_add_u32 s70, s70, 3072\n"
"s_add_u32 m0, s70, 65536\n"
"v_xor_b32 v2, 96, v1\n"
"buffer_load_dwordx4 v2, s[28:31], s70 offen offset:0 lds\n"
".Lr_170_%=:\n"
"s_add_u32 s66, s54, 8\n"
"s_mov_b32 s78, 0x5555\n"
"s_cmp_eq_u32 s48, 3\n"
"s_cselect_b32 s79, 3, 0\n"
"s_cmp_eq_u32 s48, 0\n"
"s_cbranch_scc0 .Lr_171_%=\n"
"v_and_b32 v8, 15, v0\n"
"v_lshrrev_b32 v9, 4, v0\n"
"v_lshl_add_u32 v8, v8, 1, v9\n"
"v_lshlrev_b32 v8, 3, v8\n"
"v_lshlrev_b32 v8, 1, v8\n"
"ds_read_b128 v[40:43], v8 offset:2560\n"
"v_and_b32 v1, 3, v0\n"
"v_mul_u32_u24 v1, 0x5555, v1\n"
"v_bfe_i32 v3, v0, 2, 1\n"
"v_and_b32 v3, 0x3f80, v3\n"
"v_xor_b32 v2, 0x3f80, v3\n"
"v_mov_b64 v[4:5], v[2:3]\n"
"v_and_b32 v7, 15, v0\n"
"v_min_u32 v7, 9, v7\n"
"v_mul_u32_u24 v9, 160, v9\n"
"v_lshl_add_u32 v7, v7, 4, v9\n"
"s_add_u32 s73, s58, s59\n"
"s_add_u32 s73, s73, 262144\n"
"s_add_u32 s74, s73, 2560\n"
".Lr_171_%=:\n"
"v_and_b32 v231, 15, v0\n"
"v_lshrrev_b32 v232, 4, v0\n"
"v_xor_b32 v232, v232, v231\n"
"v_mul_u32_u24 v231, 1536, v231\n"
"v_lshl_add_u32 v231, v232, 4, v231\n"
"s_and_b32 s70, s48, 1\n"
"s_lshl_b32 s70, s70, 7\n"
"s_mul_i32 s71, s48, 384\n"
"v_xor_b32 v229, s70, v231\n"
"s_andn2_b32 s72, s71, 255\n"
"s_add_u32 s72, s72, 65536\n"
"v_xor_b32 v231, 128, v229\n"
"v_add_u32 v229, s72, v229\n"
"s_add_u32 s71, s71, 128\n"
"s_andn2_b32 s72, s71, 255\n"
"s_add_u32 s72, s72, 65536\n"
"v_add_u32 v231, s72, v231\n"
"v_xor_b32 v230, 64, v229\n"
"v_xor_b32 v232, 64, v231\n"
"v_add_u32 v229, 49152, v229\n"
"v_add_u32 v230, 49152, v230\n"
"v_add_u32 v231, 49152, v231\n"
"v_add_u32 v232, 49152, v232\n"
"ds_read_b128 a[48:51], v229 offset:0\n"
"ds_read_b128 a[52:55], v230 offset:0\n"
"ds_read_b128 a[56:59], v231 offset:0\n"
"ds_read_b128 a[60:63], v232 offset:0\n"
"ds_read_b128 a[64:67], v229 offset:256\n"
"ds_read_b128 a[68:71], v230 offset:256\n"
"ds_read_b128 a[72:75], v229 offset:24576\n"
"ds_read_b128 a[76:79], v230 offset:24576\n"
"ds_read_b128 a[80:83], v231 offset:24576\n"
"ds_read_b128 a[84:87], v232 offset:24576\n"
"ds_read_b128 a[88:91], v229 offset:24832\n"
"ds_read_b128 a[92:95], v230 offset:24832\n"
"v_lshrrev_b32 v232, 1, v0\n"
"v_and_b32 v233, 15, v232\n"
"v_and_b32 v232, 30, v232\n"
"v_mul_u32_u24 v232, 1536, v232\n"
"v_lshl_add_u32 v232, v233, 4, v232\n"
"v_and_b32 v233, 1, v0\n"
"v_lshl_add_u32 v232, v233, 3, v232\n"
"s_and_b32 s70, s48, 1\n"
"s_lshl_b32 s70, s70, 7\n"
"s_mul_i32 s71, s48, 384\n"
"v_xor_b32 v232, s70, v232\n"
"s_andn2_b32 s72, s71, 255\n"
"s_add_u32 s72, s72, 65536\n"
"v_xor_b32 v233, 128, v232\n"
"v_add_u32 v232, s72, v232\n"
"s_add_u32 s71, s71, 128\n"
"s_andn2_b32 s72, s71, 255\n"
"s_add_u32 s72, s72, 65536\n"
"v_add_u32 v233, s72, v233\n"
"v_xor_b32 v228, 0, v232\n"
"v_xor_b32 v229, 16, v232\n"
"ds_read_b64_tr_b16 v[132:133], v228 offset:49152\n"
"ds_read_b64_tr_b16 v[134:135], v229 offset:50688\n"
"v_xor_b32 v230, 32, v232\n"
"v_xor_b32 v231, 48, v232\n"
"ds_read_b64_tr_b16 v[140:141], v230 offset:49152\n"
"ds_read_b64_tr_b16 v[142:143], v231 offset:50688\n"
"v_xor_b32 v228, 64, v232\n"
"v_xor_b32 v229, 80, v232\n"
"ds_read_b64_tr_b16 v[148:149], v228 offset:49152\n"
"ds_read_b64_tr_b16 v[150:151], v229 offset:50688\n"
"v_xor_b32 v230, 96, v232\n"
"v_xor_b32 v231, 112, v232\n"
"ds_read_b64_tr_b16 v[156:157], v230 offset:49152\n"
"ds_read_b64_tr_b16 v[158:159], v231 offset:50688\n"
"v_xor_b32 v228, 0, v233\n"
"v_xor_b32 v229, 16, v233\n"
"ds_read_b64_tr_b16 v[164:165], v228 offset:49152\n"
"ds_read_b64_tr_b16 v[166:167], v229 offset:50688\n"
"v_xor_b32 v230, 32, v233\n"
"v_xor_b32 v231, 48, v233\n"
"ds_read_b64_tr_b16 v[172:173], v230 offset:49152\n"
"ds_read_b64_tr_b16 v[174:175], v231 offset:50688\n"
"v_xor_b32 v228, 64, v233\n"
"v_xor_b32 v229, 80, v233\n"
"ds_read_b64_tr_b16 v[180:181], v228 offset:49152\n"
"ds_read_b64_tr_b16 v[182:183], v229 offset:50688\n"
"v_xor_b32 v230, 96, v233\n"
"v_xor_b32 v231, 112, v233\n"
"ds_read_b64_tr_b16 v[188:189], v230 offset:49152\n"
"ds_read_b64_tr_b16 v[190:191], v231 offset:50688\n"
"v_xor_b32 v228, 0, v232\n"
"v_xor_b32 v229, 16, v232\n"
"ds_read_b64_tr_b16 v[196:197], v228 offset:49408\n"
"ds_read_b64_tr_b16 v[198:199], v229 offset:50944\n"
"v_xor_b32 v230, 32, v232\n"
"v_xor_b32 v231, 48, v232\n"
"ds_read_b64_tr_b16 v[204:205], v230 offset:49408\n"
"ds_read_b64_tr_b16 v[206:207], v231 offset:50944\n"
"v_xor_b32 v228, 64, v232\n"
"v_xor_b32 v229, 80, v232\n"
"ds_read_b64_tr_b16 v[212:213], v228 offset:49408\n"
"ds_read_b64_tr_b16 v[214:215], v229 offset:50944\n"
"v_xor_b32 v230, 96, v232\n"
"v_xor_b32 v231, 112, v232\n"
"ds_read_b64_tr_b16 v[220:221], v230 offset:49408\n"
"ds_read_b64_tr_b16 v[222:223], v231 offset:50944\n"
"s_cmp_eq_u32 s48, 0\n"
"s_cbranch_scc0 .Lr_172_%=\n"
"s_sleep 6\n"
"buffer_load_dwordx4 v[8:11], v7, s[40:43], s73 offen offset:0 sc1\n"
"buffer_load_dwordx4 v[12:15], v7, s[40:43], s73 offen offset:640 sc1\n"
"buffer_load_dwordx4 v[16:19], v7, s[40:43], s73 offen offset:1280 sc1\n"
"buffer_load_dwordx4 v[20:23], v7, s[40:43], s73 offen offset:1920 sc1\n"
"buffer_load_dwordx4 v[24:27], v7, s[40:43], s74 offen offset:0 sc1\n"
"buffer_load_dwordx4 v[28:31], v7, s[40:43], s74 offen offset:640 sc1\n"
"buffer_load_dwordx4 v[32:35], v7, s[40:43], s74 offen offset:1280 sc1\n"
"buffer_load_dwordx4 v[36:39], v7, s[40:43], s74 offen offset:1920 sc1\n"
".Lr_172_%=:\n"
"s_barrier\n"
"s_cmp_lg_u32 s48, 0\n"
"s_cbranch_scc0 .Lr_173_%=\n"
"v_lshl_add_u32 v1, s48, 5, v0\n"
"v_subrev_u32 v1, 32, v1\n"
"v_lshrrev_b32 v1, 6, v1\n"
"v_xor_b32 v1, v0, v1\n"
"v_lshlrev_b32 v1, 4, v1\n"
"s_lshl_b32 s70, s48, 10\n"
"s_add_u32 s70, s70, 35840\n"
"s_add_u32 m0, s70, 65536\n"
"v_xor_b32 v2, 128, v1\n"
"buffer_load_dwordx4 v2, s[28:31], s70 offen offset:0 lds\n"
"s_add_u32 s70, s70, 3072\n"
"s_add_u32 m0, s70, 65536\n"
"v_xor_b32 v2, 160, v1\n"
"buffer_load_dwordx4 v2, s[28:31], s70 offen offset:0 lds\n"
"s_add_u32 s70, s70, 3072\n"
"s_add_u32 m0, s70, 65536\n"
"v_xor_b32 v2, 192, v1\n"
"buffer_load_dwordx4 v2, s[28:31], s70 offen offset:0 lds\n"
"s_add_u32 s70, s70, 3072\n"
"s_add_u32 m0, s70, 65536\n"
"v_xor_b32 v2, 224, v1\n"
"buffer_load_dwordx4 v2, s[28:31], s70 offen offset:0 lds\n"
".Lr_173_%=:\n"
"s_cmp_lg_u32 s48, 0\n"
"s_cbranch_scc0 .Lr_174_%=\n"
"v_and_b32 v2, 1, v0\n"
"v_mul_u32_u24 v2, 160, v2\n"
"v_lshrrev_b32 v3, 4, v0\n"
"v_lshl_add_u32 v1, v3, 4, v2\n"
"v_lshl_add_u32 v2, v3, 3, v2\n"
"s_waitcnt lgkmcnt(0)\n"
".Lr_174_%=:\n"
"s_cmp_eq_u32 s48, 0\n"
"s_cbranch_scc0 .Lr_175_%=\n"
".Lgb_176_%=:\n"
"s_waitcnt lgkmcnt(0)\n"
"s_waitcnt vmcnt(6)\n"
"v_smfmac_f32_16x16x64_bf16 v[40:43], v[2:5], v[8:15], v1\n"
"v_max_u32 v6, v8, v9\n"
"v_max3_u32 v6, v6, v10, v11\n"
"v_max3_u32 v6, v6, v12, v13\n"
"v_max3_u32 v6, v6, v14, v15\n"
"s_waitcnt vmcnt(4)\n"
"v_smfmac_f32_16x16x64_bf16 v[40:43], v[2:5], v[16:23], v1\n"
"v_max3_u32 v6, v6, v16, v17\n"
"v_max3_u32 v6, v6, v18, v19\n"
"v_max3_u32 v6, v6, v20, v21\n"
"v_max3_u32 v6, v6, v22, v23\n"
"v_and_b32 v8, 15, v0\n"
"v_lshrrev_b32 v9, 4, v0\n"
"v_lshl_add_u32 v8, v8, 1, v9\n"
"v_lshlrev_b32 v8, 3, v8\n"
"s_waitcnt vmcnt(2)\n"
"v_smfmac_f32_16x16x64_bf16 v[40:43], v[2:5], v[24:31], v1\n"
"v_max3_u32 v6, v6, v24, v25\n"
"v_max3_u32 v6, v6, v26, v27\n"
"v_max3_u32 v6, v6, v28, v29\n"
"v_max3_u32 v6, v6, v30, v31\n"
"s_waitcnt vmcnt(0)\n"
"v_smfmac_f32_16x16x64_bf16 v[40:43], v[2:5], v[32:39], v1\n"
"v_max3_u32 v6, v6, v32, v33\n"
"v_max3_u32 v6, v6, v34, v35\n"
"v_max3_u32 v6, v6, v36, v37\n"
"v_max3_u32 v6, v6, v38, v39\n"
"v_cmp_eq_u32 vcc, -1, v6\n"
"s_mov_b64 exec, 0x03ff03ff\n"
"s_nop 1\n"
"v_cvt_pk_bf16_f32 v10, v40, v41\n"
"v_cvt_pk_bf16_f32 v11, v42, v43\n"
"ds_write_b64 v8, v[10:11] offset:0\n"
"s_mov_b64 exec, -1\n"
"s_cbranch_vccnz .Lgr_177_%=\n"
".Lgo_178_%=:\n"
"v_and_b32 v2, 1, v0\n"
"v_mul_u32_u24 v2, 160, v2\n"
"v_lshrrev_b32 v3, 4, v0\n"
"v_lshl_add_u32 v1, v3, 4, v2\n"
"v_lshl_add_u32 v2, v3, 3, v2\n"
"s_waitcnt lgkmcnt(0)\n"
".Lr_175_%=:\n"
"s_barrier\n"
"ds_read_b128 v[18:21], v1 offset:0\n"
"ds_read_b128 v[22:25], v1 offset:64\n"
"ds_read_b64 v[26:27], v2 offset:128\n"
"s_cmp_eq_u32 s48, 0\n"
"s_cbranch_scc0 .Lr_179_%=\n"
"v_lshlrev_b32 v12, 1, v8\n"
"s_mov_b64 exec, 0x03ff03ff\n"
"ds_write_b128 v12, v[40:43] offset:2560\n"
"s_mov_b64 exec, -1\n"
".Lr_179_%=:\n"
"s_lshl2_add_u32 s70, s50, s48\n"
"s_lshl_b32 s70, s70, 7\n"
"v_lshl_add_u32 v16, v0, 3, s70\n"
"s_lshl_b32 s70, s50, 7\n"
"s_add_u32 s70, s70, 3584\n"
"s_mov_b32 exec_lo, 0\n"
"v_lshl_add_u32 v16, v0, 4, s70\n"
"s_mov_b32 exec_lo, -1\n"
"v_mov_b32 v11, s66\n"
"s_waitcnt lgkmcnt(2)\n"
"v_mfma_f32_16x16x32_bf16 v[28:31], v[18:21], v[68:71], 0\n"
"v_mfma_f32_16x16x32_bf16 v[32:35], v[18:21], v[78:81], 0\n"
"v_mfma_f32_16x16x32_bf16 v[36:39], v[18:21], v[88:91], 0\n"
"v_mfma_f32_16x16x32_bf16 v[40:43], v[18:21], v[18:21], 0\n"
"s_waitcnt lgkmcnt(1)\n"
"v_mfma_f32_16x16x32_bf16 v[28:31], v[22:25], v[72:75], v[28:31]\n"
"v_mfma_f32_16x16x32_bf16 v[32:35], v[22:25], v[82:85], v[32:35]\n"
"v_mfma_f32_16x16x32_bf16 v[36:39], v[22:25], v[92:95], v[36:39]\n"
"v_mfma_f32_16x16x32_bf16 v[40:43], v[22:25], v[22:25], v[40:43]\n"
"s_waitcnt lgkmcnt(0)\n"
"v_mfma_f32_16x16x16_bf16 v[28:31], v[26:27], v[76:77], v[28:31]\n"
"v_mfma_f32_16x16x16_bf16 v[32:35], v[26:27], v[86:87], v[32:35]\n"
"v_mfma_f32_16x16x16_bf16 v[36:39], v[26:27], v[96:97], v[36:39]\n"
"v_mfma_f32_16x16x16_bf16 v[40:43], v[26:27], v[26:27], v[40:43]\n"
"v_mov_b32_dpp v29, v28 quad_perm:[1,1,3,3] row_mask:0xf bank_mask:0xf\n"
"v_mov_b32_dpp v33, v32 quad_perm:[1,1,3,3] row_mask:0xf bank_mask:0xf\n"
"s_nop 1\n"
"v_mov_b32_dpp v37, v36 quad_perm:[1,1,3,3] row_mask:0xf bank_mask:0xf\n"
"v_cvt_pk_bf16_f32 v8, v28, v29\n"
"v_cvt_pk_bf16_f32 v9, v32, v33\n"
"v_cvt_pk_bf16_f32 v10, v36, v37\n"
"v_mov_b32_dpp v8, v40 quad_perm:[0,1,2,3] row_mask:0x4 bank_mask:0xf\n"
"s_mov_b64 exec, s[78:79]\n"
"buffer_store_dwordx4 v[8:11], v16, s[40:43], s56 offen offset:0 sc1\n"
"s_mov_b64 exec, -1\n"
"v_and_b32 v6, 1, v0\n"
"v_mul_u32_u24 v6, 0xaaaa, v6\n"
"v_add_u32 v6, 0x4444, v6\n"
"s_cmp_eq_u32 s48, 0\n"
"s_cbranch_scc0 .Lr_180_%=\n"
"v_and_b32 v38, 15, v0\n"
"v_lshlrev_b32 v38, 1, v38\n"
"s_mov_b32 s70, 0x44ee44\n"
"v_bfe_u32 v1, s70, v38, 2\n"
"s_mov_b32 s70, 0xf5a0a0\n"
"v_bfe_u32 v38, s70, v38, 2\n"
"v_mul_u32_u24 v1, 5, v1\n"
"v_lshlrev_b32 v2, 2, v38\n"
"v_lshlrev_b32 v1, v2, v1\n"
"v_lshlrev_b32 v38, v38, 1\n"
"v_bfe_i32 v2, v38, 0, 1\n"
"v_and_b32 v2, 0x3f80, v2\n"
"v_bfe_i32 v3, v38, 1, 1\n"
"v_and_b32 v3, 0x3f80, v3\n"
"v_bfe_i32 v4, v38, 2, 1\n"
"v_and_b32 v4, 0x3f80, v4\n"
"v_bfe_i32 v5, v38, 3, 1\n"
"v_and_b32 v5, 0x3f80, v5\n"
"v_and_b32 v7, 15, v0\n"
"v_bfe_u32 v38, v0, 4, 1\n"
"v_lshl_add_u32 v7, v7, 1, v38\n"
"v_and_b32 v38, 32, v0\n"
"v_add_lshl_u32 v7, v7, v38, 4\n"
"v_mov_b64 v[28:29], 0\n"
"v_mov_b64 v[30:31], 0\n"
"v_bfe_u32 v38, v0, 2, 2\n"
"v_lshrrev_b32 v36, 4, v0\n"
"v_mad_u32_u24 v38, v38, 3, v36\n"
"v_and_b32 v36, 3, v0\n"
"v_lshl_add_u32 v36, v38, 3, v36\n"
"v_lshlrev_b32 v36, 2, v36\n"
"v_lshrrev_b32 v42, 4, v0\n"
"v_and_b32 v38, 1, v0\n"
"v_lshl_add_u32 v42, v42, 1, v38\n"
"v_lshlrev_b32 v42, 4, v42\n"
"v_and_b32 v37, 14, v0\n"
"v_and_b32 v39, 1, v0\n"
"v_lshl_add_u32 v37, v37, 2, v39\n"
"v_lshlrev_b32 v37, 4, v37\n"
"v_add_u32 v37, 4096, v37\n"
"v_lshlrev_b32 v39, 4, v0\n"
"ds_read_b128 v[32:35], v39 offset:1792\n"
".Lr_180_%=:\n"
"s_cmp_lg_u32 s48, 0\n"
"s_cbranch_scc0 .Lr_181_%=\n"
"s_waitcnt vmcnt(1)\n"
".Lr_181_%=:\n"
"s_barrier\n"
"v_and_b32 v231, 15, v0\n"
"v_lshrrev_b32 v232, 4, v0\n"
"v_xor_b32 v232, v232, v231\n"
"v_mul_u32_u24 v231, 1536, v231\n"
"v_lshl_add_u32 v231, v232, 4, v231\n"
"s_and_b32 s70, s48, 1\n"
"s_lshl_b32 s70, s70, 7\n"
"s_mul_i32 s71, s48, 384\n"
"v_xor_b32 v229, s70, v231\n"
"s_andn2_b32 s72, s71, 255\n"
"s_add_u32 s72, s72, 65536\n"
"v_xor_b32 v231, 128, v229\n"
"v_add_u32 v229, s72, v229\n"
"s_add_u32 s71, s71, 128\n"
"s_andn2_b32 s72, s71, 255\n"
"s_add_u32 s72, s72, 65536\n"
"v_add_u32 v231, s72, v231\n"
"v_xor_b32 v230, 64, v229\n"
"v_xor_b32 v232, 64, v231\n"
"ds_read_b128 a[0:3], v229 offset:0\n"
"ds_read_b128 a[4:7], v230 offset:0\n"
"ds_read_b128 a[8:11], v231 offset:0\n"
"ds_read_b128 a[12:15], v232 offset:0\n"
"ds_read_b128 a[16:19], v229 offset:256\n"
"ds_read_b128 a[20:23], v230 offset:256\n"
"ds_read_b128 a[24:27], v229 offset:24576\n"
"ds_read_b128 a[28:31], v230 offset:24576\n"
"ds_read_b128 a[32:35], v231 offset:24576\n"
"ds_read_b128 a[36:39], v232 offset:24576\n"
"ds_read_b128 a[40:43], v229 offset:24832\n"
"ds_read_b128 a[44:47], v230 offset:24832\n"
"s_cmp_eq_u32 s48, 0\n"
"s_cbranch_scc0 .Lr_182_%=\n"
"s_sleep 3\n"
"buffer_load_dwordx4 v[8:11], v7, s[40:43], s56 offen offset:0 sc1\n"
"buffer_load_dwordx4 v[12:15], v7, s[40:43], s56 offen offset:1024 sc1\n"
"buffer_load_dwordx4 v[16:19], v7, s[40:43], s56 offen offset:2048 sc1\n"
"buffer_load_dwordx4 v[20:23], v7, s[40:43], s56 offen offset:3072 sc1\n"
"buffer_load_dwordx4 v[24:27], v37, s[40:43], s56 offen offset:0 sc1\n"
".Lr_182_%=:\n"
"s_cmp_lg_u32 s48, 0\n"
"s_cbranch_scc0 .Lr_183_%=\n"
"v_lshrrev_b32 v42, 4, v0\n"
"v_and_b32 v38, 1, v0\n"
"v_lshl_add_u32 v42, v42, 1, v38\n"
"v_lshlrev_b32 v42, 4, v42\n"
".Lr_183_%=:\n"
"s_cmp_eq_u32 s48, 0\n"
"s_cbranch_scc0 .Lr_184_%=\n"
".Lgb_185_%=:\n"
"s_waitcnt vmcnt(3)\n"
"v_smfmac_f32_16x16x64_bf16 v[28:31], v[2:5], v[8:15], v1\n"
"s_waitcnt vmcnt(1)\n"
"v_smfmac_f32_16x16x64_bf16 v[28:31], v[2:5], v[16:23], v1\n"
"s_waitcnt vmcnt(0)\n"
"v_add_f32_dpp v24, v24, v24 row_ror:2 row_mask:0xf bank_mask:0xf\n"
"v_bitop3_b32 v38, v11, v15, s66 bitop3:0x7e\n"
"v_bitop3_b32 v38, v19, v38, s66 bitop3:0xde\n"
"v_add_f32_dpp v24, v24, v24 row_ror:4 row_mask:0xf bank_mask:0xf\n"
"v_bitop3_b32 v38, v23, v38, s66 bitop3:0xde\n"
"v_bitop3_b32 v38, v27, v38, s66 bitop3:0xde\n"
"v_add_f32_dpp v24, v24, v24 row_ror:8 row_mask:0xf bank_mask:0xf\n"
"v_cmp_ne_u32 vcc, 0, v38\n"
"s_waitcnt lgkmcnt(0)\n"
"v_fmac_f32_dpp v28, v24, v32 quad_perm:[1,1,3,3] row_mask:0xf bank_mask:0xf\n"
"v_fmac_f32_dpp v29, v24, v33 quad_perm:[1,1,3,3] row_mask:0xf bank_mask:0xf\n"
"v_fmac_f32_dpp v30, v24, v34 quad_perm:[1,1,3,3] row_mask:0xf bank_mask:0xf\n"
"v_fmac_f32_dpp v31, v24, v35 quad_perm:[1,1,3,3] row_mask:0xf bank_mask:0xf\n"
"v_cvt_pk_bf16_f32 v40, v28, v29\n"
"v_cvt_pk_bf16_f32 v41, v30, v31\n"
"s_mov_b32 exec_hi, 0xffff\n"
"ds_write2_b32 v36, v40, v41 offset0:96 offset1:100\n"
"s_mov_b64 exec, 3\n"
"ds_write_b32 v39, v24 offset:320\n"
"s_mov_b64 exec, -1\n"
"s_cbranch_vccnz .Lgr_186_%=\n"
".Lgo_187_%=:\n"
".Lr_184_%=:\n"
"s_waitcnt lgkmcnt(0)\n"
"s_barrier\n"
"s_cmp_lg_u32 s48, 0\n"
"s_cbranch_scc0 .Lr_188_%=\n"
"ds_read_b128 v[8:11], v42 offset:384\n"
"ds_read_b128 v[12:15], v42 offset:512\n"
"ds_read_b128 v[16:19], v42 offset:640\n"
"v_mov_b32 v7, 0\n"
"ds_read2_b32 v[42:43], v7 offset0:80 offset1:84\n"
"v_mov_b64 v[2:3], 0\n"
"v_mov_b64 v[36:37], 0\n"
"v_lshlrev_b32 v1, 2, v0\n"
"s_waitcnt lgkmcnt(3)\n"
"v_smfmac_f32_16x16x64_bf16 v[2:5], v[8:11], a[96:103], v6\n"
"v_smfmac_f32_16x16x64_bf16 v[36:39], v[8:11], a[120:127], v6\n"
"s_waitcnt lgkmcnt(2)\n"
"v_smfmac_f32_16x16x64_bf16 v[2:5], v[12:15], a[104:111], v6\n"
"v_smfmac_f32_16x16x64_bf16 v[36:39], v[12:15], a[128:135], v6\n"
"s_waitcnt lgkmcnt(1)\n"
"v_smfmac_f32_16x16x64_bf16 v[2:5], v[16:19], a[112:119], v6\n"
"v_smfmac_f32_16x16x64_bf16 v[36:39], v[16:19], a[136:143], v6\n"
"s_waitcnt lgkmcnt(0)\n"
"v_mul_f32 v43, 0x3acccccd, v43\n"
"v_mul_f32 v42, 0x3acccccd, v42\n"
"v_fma_f32 v42, -v43, v43, v42\n"
"v_max_f32 v42, 0, v42\n"
"v_add_f32 v42, 0x3727c5ac, v42\n"
"v_rsq_f32 v42, v42\n"
"s_nop 0\n"
"v_mul_f32 v42, 0x3dd53b95, v42\n"
"s_mov_b64 exec, 0xffff\n"
"v_add_f32 v2, v2, v3\n"
"v_add_f32 v36, v36, v37\n"
"v_mul_f32 v2, v2, v42\n"
"v_mul_f32 v36, v36, v42\n"
"v_cvt_pk_bf16_f32 v2, v2, v36\n"
"buffer_store_dword v2, v1, s[40:43], s61 offen offset:0 sc1\n"
"s_mov_b64 exec, -1\n"
".Lr_188_%=:\n"
"s_cmp_eq_u32 s48, 0\n"
"s_cbranch_scc0 .Lr_189_%=\n"
"s_sleep 10\n"
".Lr_189_%=:\n"
"v_bfe_u32 v5, v0, 1, 3\n"
"v_mul_u32_u24 v5, 768, v5\n"
"v_and_b32 v7, 48, v0\n"
"v_lshl_add_u32 v7, v7, 1, v5\n"
"v_and_b32 v5, 1, v0\n"
"v_lshl_add_u32 v7, v5, 4, v7\n"
"s_mul_i32 s70, s48, 2304\n"
"s_add_u32 s70, s70, 10240\n"
"v_lshrrev_b32 v5, 4, v0\n"
"v_mul_u32_u24 v5, 144, v5\n"
"v_and_or_b32 v5, v0, 15, v5\n"
"v_lshl_add_u32 v5, v5, 2, s70\n"
"v_mov_b64 v[20:21], 0\n"
"v_mov_b64 v[22:23], 0\n"
"v_mov_b64 v[44:45], 0\n"
"v_mov_b64 v[46:47], 0\n"
"v_mov_b64 v[48:49], 0\n"
"v_mov_b64 v[50:51], 0\n"
"v_mov_b64 v[52:53], 0\n"
"v_mov_b64 v[54:55], 0\n"
"v_mov_b64 v[56:57], 0\n"
"v_mov_b64 v[58:59], 0\n"
"v_mov_b64 v[60:61], 0\n"
"v_mov_b64 v[62:63], 0\n"
"v_mov_b64 v[64:65], 0\n"
"v_mov_b64 v[66:67], 0\n"
"v_mov_b64 v[68:69], 0\n"
"v_mov_b64 v[70:71], 0\n"
"v_mov_b64 v[72:73], 0\n"
"v_mov_b64 v[74:75], 0\n"
"v_mov_b64 v[76:77], 0\n"
"v_mov_b64 v[78:79], 0\n"
"v_mov_b64 v[80:81], 0\n"
"v_mov_b64 v[82:83], 0\n"
"v_mov_b64 v[84:85], 0\n"
"v_mov_b64 v[86:87], 0\n"
"v_mov_b64 v[88:89], 0\n"
"v_mov_b64 v[90:91], 0\n"
"v_mov_b64 v[92:93], 0\n"
"v_mov_b64 v[94:95], 0\n"
"v_mov_b64 v[96:97], 0\n"
"v_mov_b64 v[98:99], 0\n"
"v_mov_b64 v[100:101], 0\n"
"v_mov_b64 v[102:103], 0\n"
"v_mov_b64 v[104:105], 0\n"
"v_mov_b64 v[106:107], 0\n"
"v_bfe_u32 v108, v0, 1, 3\n"
"v_mul_u32_u24 v108, 18, v108\n"
"v_and_or_b32 v108, v0, 1, v108\n"
"v_and_b32 v1, 48, v0\n"
"v_lshlrev_b32 v1, 2, v1\n"
"v_lshl_add_u32 v108, v108, 4, v1\n"
"v_and_b32 v109, 15, v0\n"
"v_and_b32 v1, 48, v0\n"
"v_lshlrev_b32 v1, 15, v1\n"
"v_lshl_add_u32 v109, v109, 4, v1\n"
"s_add_u32 s66, s54, 9\n"
"v_mov_b32 v114, s66\n"
"v_mov_b32 v119, s66\n"
"v_mov_b32 v123, s66\n"
"v_mov_b32 v127, s66\n"
"v_mov_b32 v227, s66\n"
"v_lshlrev_b32 v110, 13, v0\n"
"s_add_u32 s75, s63, 262144\n"
"s_add_u32 s76, s63, 32768\n"
"s_add_u32 s77, s75, 32768\n"
"v_lshrrev_b32 v232, 1, v0\n"
"v_and_b32 v233, 15, v232\n"
"v_and_b32 v232, 30, v232\n"
"v_mul_u32_u24 v232, 1536, v232\n"
"v_lshl_add_u32 v232, v233, 4, v232\n"
"v_and_b32 v233, 1, v0\n"
"v_lshl_add_u32 v232, v233, 3, v232\n"
"s_and_b32 s70, s48, 1\n"
"s_lshl_b32 s70, s70, 7\n"
"s_mul_i32 s71, s48, 384\n"
"v_xor_b32 v232, s70, v232\n"
"s_andn2_b32 s72, s71, 255\n"
"s_add_u32 s72, s72, 65536\n"
"v_xor_b32 v233, 128, v232\n"
"v_add_u32 v232, s72, v232\n"
"s_add_u32 s71, s71, 128\n"
"s_andn2_b32 s72, s71, 255\n"
"s_add_u32 s72, s72, 65536\n"
"v_add_u32 v233, s72, v233\n"
"v_xor_b32 v228, 0, v232\n"
"v_xor_b32 v229, 16, v232\n"
"ds_read_b64_tr_b16 v[128:129], v228 offset:0\n"
"ds_read_b64_tr_b16 v[130:131], v229 offset:1536\n"
"v_xor_b32 v230, 32, v232\n"
"v_xor_b32 v231, 48, v232\n"
"ds_read_b64_tr_b16 v[136:137], v230 offset:0\n"
"ds_read_b64_tr_b16 v[138:139], v231 offset:1536\n"
"v_xor_b32 v228, 64, v232\n"
"v_xor_b32 v229, 80, v232\n"
"ds_read_b64_tr_b16 v[144:145], v228 offset:0\n"
"ds_read_b64_tr_b16 v[146:147], v229 offset:1536\n"
"v_xor_b32 v230, 96, v232\n"
"v_xor_b32 v231, 112, v232\n"
"ds_read_b64_tr_b16 v[152:153], v230 offset:0\n"
"ds_read_b64_tr_b16 v[154:155], v231 offset:1536\n"
"v_xor_b32 v228, 0, v233\n"
"v_xor_b32 v229, 16, v233\n"
"ds_read_b64_tr_b16 v[160:161], v228 offset:0\n"
"ds_read_b64_tr_b16 v[162:163], v229 offset:1536\n"
"v_xor_b32 v230, 32, v233\n"
"v_xor_b32 v231, 48, v233\n"
"ds_read_b64_tr_b16 v[168:169], v230 offset:0\n"
"ds_read_b64_tr_b16 v[170:171], v231 offset:1536\n"
"v_xor_b32 v228, 64, v233\n"
"v_xor_b32 v229, 80, v233\n"
"ds_read_b64_tr_b16 v[176:177], v228 offset:0\n"
"ds_read_b64_tr_b16 v[178:179], v229 offset:1536\n"
"v_xor_b32 v230, 96, v233\n"
"v_xor_b32 v231, 112, v233\n"
"ds_read_b64_tr_b16 v[184:185], v230 offset:0\n"
"ds_read_b64_tr_b16 v[186:187], v231 offset:1536\n"
"v_xor_b32 v228, 0, v232\n"
"v_xor_b32 v229, 16, v232\n"
"ds_read_b64_tr_b16 v[192:193], v228 offset:256\n"
"ds_read_b64_tr_b16 v[194:195], v229 offset:1792\n"
"v_xor_b32 v230, 32, v232\n"
"v_xor_b32 v231, 48, v232\n"
"ds_read_b64_tr_b16 v[200:201], v230 offset:256\n"
"ds_read_b64_tr_b16 v[202:203], v231 offset:1792\n"
"v_xor_b32 v228, 64, v232\n"
"v_xor_b32 v229, 80, v232\n"
"ds_read_b64_tr_b16 v[208:209], v228 offset:256\n"
"ds_read_b64_tr_b16 v[210:211], v229 offset:1792\n"
"v_xor_b32 v230, 96, v232\n"
"v_xor_b32 v231, 112, v232\n"
"ds_read_b64_tr_b16 v[216:217], v230 offset:256\n"
"ds_read_b64_tr_b16 v[218:219], v231 offset:1792\n"
"buffer_load_dwordx4 v[8:11], v7, s[40:43], s60 offen offset:0 sc1\n"
"buffer_load_dwordx4 v[12:15], v7, s[40:43], s60 offen offset:128 sc1\n"
"buffer_load_dwordx4 v[16:19], v7, s[40:43], s60 offen offset:256 sc1\n"
"s_waitcnt lgkmcnt(0)\n"
".Lgb_190_%=:\n"
"s_waitcnt vmcnt(0)\n"
"v_max3_u32 v24, v8, v9, v10\n"
"v_max3_u32 v25, v11, v12, v13\n"
"v_max3_u32 v24, v24, v14, v15\n"
"v_max3_u32 v25, v25, v16, v17\n"
"v_max3_u32 v24, v24, v18, v19\n"
"v_max_u32 v24, v24, v25\n"
"v_cmp_eq_u32 vcc, -1, v24\n"
"s_cbranch_vccnz .Lgr_191_%=\n"
".Lgo_192_%=:\n"
"v_smfmac_f32_16x16x64_bf16 v[92:95], v[8:11], a[0:7], v6\n"
"v_smfmac_f32_16x16x64_bf16 v[92:95], v[12:15], a[8:15], v6\n"
"v_smfmac_f32_16x16x64_bf16 v[92:95], v[16:19], a[16:23], v6\n"
"v_smfmac_f32_16x16x64_bf16 v[96:99], v[8:11], a[24:31], v6\n"
"v_smfmac_f32_16x16x64_bf16 v[96:99], v[12:15], a[32:39], v6\n"
"v_smfmac_f32_16x16x64_bf16 v[96:99], v[16:19], a[40:47], v6\n"
"v_add_f32 v92, v92, v93\n"
"v_add_f32 v94, v94, v95\n"
"ds_write2_b32 v5, v92, v94 offset0:0 offset1:72\n"
"v_smfmac_f32_16x16x64_bf16 v[100:103], v[8:11], a[48:55], v6\n"
"v_smfmac_f32_16x16x64_bf16 v[100:103], v[12:15], a[56:63], v6\n"
"v_smfmac_f32_16x16x64_bf16 v[100:103], v[16:19], a[64:71], v6\n"
"v_add_f32 v96, v96, v97\n"
"v_add_f32 v98, v98, v99\n"
"ds_write2_b32 v5, v96, v98 offset0:16 offset1:88\n"
"v_smfmac_f32_16x16x64_bf16 v[104:107], v[8:11], a[72:79], v6\n"
"v_smfmac_f32_16x16x64_bf16 v[104:107], v[12:15], a[80:87], v6\n"
"v_smfmac_f32_16x16x64_bf16 v[104:107], v[16:19], a[88:95], v6\n"
"v_add_f32 v100, v100, v101\n"
"v_add_f32 v102, v102, v103\n"
"ds_write2_b32 v5, v100, v102 offset0:32 offset1:104\n"
"s_nop 4\n"
"v_add_f32 v104, v104, v105\n"
"v_add_f32 v106, v106, v107\n"
"ds_write2_b32 v5, v104, v106 offset0:48 offset1:120\n"
"s_waitcnt lgkmcnt(0)\n"
"s_barrier\n"
"ds_read_b128 v[8:11], v108 offset:10240\n"
"ds_read_b128 v[12:15], v108 offset:10272\n"
"ds_read_b128 v[16:19], v108 offset:12544\n"
"ds_read_b128 v[20:23], v108 offset:12576\n"
"ds_read_b128 v[24:27], v108 offset:14848\n"
"ds_read_b128 v[28:31], v108 offset:14880\n"
"ds_read_b128 v[32:35], v108 offset:17152\n"
"ds_read_b128 v[36:39], v108 offset:17184\n"
"s_mov_b32 s71, 0x3f803f80\n"
"s_waitcnt lgkmcnt(4)\n"
"v_pk_add_f32 v[8:9], v[8:9], v[16:17]\n"
"v_pk_add_f32 v[10:11], v[10:11], v[18:19]\n"
"v_pk_add_f32 v[12:13], v[12:13], v[20:21]\n"
"v_pk_add_f32 v[14:15], v[14:15], v[22:23]\n"
"s_waitcnt lgkmcnt(2)\n"
"v_pk_add_f32 v[8:9], v[8:9], v[24:25]\n"
"v_pk_add_f32 v[10:11], v[10:11], v[26:27]\n"
"v_pk_add_f32 v[12:13], v[12:13], v[28:29]\n"
"v_pk_add_f32 v[14:15], v[14:15], v[30:31]\n"
"s_waitcnt lgkmcnt(0)\n"
"v_pk_add_f32 v[8:9], v[8:9], v[32:33]\n"
"v_pk_add_f32 v[10:11], v[10:11], v[34:35]\n"
"v_pk_add_f32 v[12:13], v[12:13], v[36:37]\n"
"v_pk_add_f32 v[14:15], v[14:15], v[38:39]\n"
"v_max3_f32 v3, v8, v9, v10\n"
"v_max3_f32 v3, v3, v11, v12\n"
"v_max3_f32 v3, v3, v13, v14\n"
"v_max_f32 v3, v3, v15\n"
"s_nop 1\n"
"v_max_f32_dpp v112, v3, v3 quad_perm:[1,0,3,2] row_mask:0xf bank_mask:0xf\n"
"v_max_f32_dpp v3, v3, v3 quad_perm:[1,0,3,2] row_mask:0xf bank_mask:0xf\n"
"s_nop 1\n"
"v_permlane16_swap_b32 v112, v3\n"
"v_max_f32 v112, v112, v3\n"
"v_mov_b32 v3, v112\n"
"s_nop 1\n"
"v_permlane32_swap_b32 v112, v3\n"
"v_max_f32 v112, v112, v3\n"
"v_pk_add_f32 v[8:9], v[8:9], v[112:113] op_sel_hi:[1,0] neg_lo:[0,1] neg_hi:[0,1]\n"
"v_pk_add_f32 v[10:11], v[10:11], v[112:113] op_sel_hi:[1,0] neg_lo:[0,1] neg_hi:[0,1]\n"
"v_pk_add_f32 v[12:13], v[12:13], v[112:113] op_sel_hi:[1,0] neg_lo:[0,1] neg_hi:[0,1]\n"
"v_pk_add_f32 v[14:15], v[14:15], v[112:113] op_sel_hi:[1,0] neg_lo:[0,1] neg_hi:[0,1]\n"
"v_exp_f32 v8, v8\n"
"v_exp_f32 v10, v10\n"
"v_exp_f32 v9, v9\n"
"v_exp_f32 v11, v11\n"
"v_exp_f32 v12, v12\n"
"v_exp_f32 v14, v14\n"
"v_exp_f32 v13, v13\n"
"v_exp_f32 v15, v15\n"
"v_cvt_pk_bf16_f32 v40, v8, v10\n"
"v_cvt_pk_bf16_f32 v41, v9, v11\n"
"v_cvt_pk_bf16_f32 v42, v12, v14\n"
"v_cvt_pk_bf16_f32 v43, v13, v15\n"
"s_nop 1\n"
"v_smfmac_f32_16x16x64_bf16 v[44:47], v[40:43], v[128:135], v6\n"
"s_cmp_eq_u32 s48, 0\n"
"s_cbranch_scc0 .Lr_193_%=\n"
"v_dot2_f32_bf16 v113, v40, s71, 0\n"
"v_dot2_f32_bf16 v113, v41, s71, v113\n"
"v_dot2_f32_bf16 v113, v42, s71, v113\n"
"v_dot2_f32_bf16 v113, v43, s71, v113\n"
"s_nop 2\n"
"v_add_f32_dpp v113, v113, v113 quad_perm:[1,0,3,2] row_mask:0xf bank_mask:0xf\n"
"v_mov_b32 v35, v113\n"
"s_nop 1\n"
"v_permlane16_swap_b32 v113, v35\n"
"v_add_f32 v113, v113, v35\n"
"v_mov_b32 v35, v113\n"
"s_nop 1\n"
"v_permlane32_swap_b32 v113, v35\n"
"v_add_f32 v113, v113, v35\n"
"s_mov_b64 exec, 0x5555\n"
"buffer_store_dwordx3 v[112:114], v110, s[40:43], s64 offen offset:0 sc1\n"
"s_mov_b64 exec, -1\n"
".Lr_193_%=:\n"
"v_smfmac_f32_16x16x64_bf16 v[48:51], v[40:43], v[136:143], v6\n"
"v_smfmac_f32_16x16x64_bf16 v[52:55], v[40:43], v[144:151], v6\n"
"s_nop 0\n"
"v_add_f32 v116, v44, v45\n"
"v_add_f32 v120, v46, v47\n"
"v_smfmac_f32_16x16x64_bf16 v[56:59], v[40:43], v[152:159], v6\n"
"v_add_f32 v48, v48, v49\n"
"v_add_f32 v50, v50, v51\n"
"v_cvt_pk_bf16_f32 v116, v116, v48\n"
"v_cvt_pk_bf16_f32 v120, v120, v50\n"
"v_smfmac_f32_16x16x64_bf16 v[60:63], v[40:43], v[160:167], v6\n"
"v_add_f32 v117, v52, v53\n"
"v_add_f32 v121, v54, v55\n"
"v_smfmac_f32_16x16x64_bf16 v[64:67], v[40:43], v[168:175], v6\n"
"v_add_f32 v56, v56, v57\n"
"v_add_f32 v58, v58, v59\n"
"v_cvt_pk_bf16_f32 v117, v117, v56\n"
"v_cvt_pk_bf16_f32 v121, v121, v58\n"
"v_smfmac_f32_16x16x64_bf16 v[68:71], v[40:43], v[176:183], v6\n"
"v_add_f32 v118, v60, v61\n"
"v_add_f32 v122, v62, v63\n"
"v_smfmac_f32_16x16x64_bf16 v[72:75], v[40:43], v[184:191], v6\n"
"v_add_f32 v64, v64, v65\n"
"v_add_f32 v66, v66, v67\n"
"v_cvt_pk_bf16_f32 v118, v118, v64\n"
"v_cvt_pk_bf16_f32 v122, v122, v66\n"
"buffer_store_dwordx4 v[116:119], v109, s[40:43], s63 offen offset:0 sc1\n"
"buffer_store_dwordx4 v[120:123], v109, s[40:43], s75 offen offset:0 sc1\n"
"v_smfmac_f32_16x16x64_bf16 v[76:79], v[40:43], v[192:199], v6\n"
"v_add_f32 v124, v68, v69\n"
"v_add_f32 v224, v70, v71\n"
"v_smfmac_f32_16x16x64_bf16 v[80:83], v[40:43], v[200:207], v6\n"
"v_add_f32 v72, v72, v73\n"
"v_add_f32 v74, v74, v75\n"
"v_cvt_pk_bf16_f32 v124, v124, v72\n"
"v_cvt_pk_bf16_f32 v224, v224, v74\n"
"v_smfmac_f32_16x16x64_bf16 v[84:87], v[40:43], v[208:215], v6\n"
"v_add_f32 v125, v76, v77\n"
"v_add_f32 v225, v78, v79\n"
"v_smfmac_f32_16x16x64_bf16 v[88:91], v[40:43], v[216:223], v6\n"
"v_add_f32 v80, v80, v81\n"
"v_add_f32 v82, v82, v83\n"
"v_cvt_pk_bf16_f32 v125, v125, v80\n"
"v_cvt_pk_bf16_f32 v225, v225, v82\n"
"v_add_f32 v126, v84, v85\n"
"v_add_f32 v226, v86, v87\n"
"s_nop 1\n"
"v_add_f32 v88, v88, v89\n"
"v_add_f32 v90, v90, v91\n"
"v_cvt_pk_bf16_f32 v126, v126, v88\n"
"v_cvt_pk_bf16_f32 v226, v226, v90\n"
"buffer_store_dwordx4 v[124:127], v109, s[40:43], s76 offen offset:0 sc1\n"
"buffer_store_dwordx4 v[224:227], v109, s[40:43], s77 offen offset:0 sc1\n"
"v_bfe_i32 v3, v0, 3, 1\n"
"v_not_b32 v2, v3\n"
"v_bfe_u32 v1, v0, 2, 1\n"
"v_and_b32 v4, 1, v0\n"
"v_lshl_add_u32 v1, v1, 1, v4\n"
"v_mul_u32_u24 v1, 0x505, v1\n"
"v_mul_u32_u24 v5, 0x5050, v4\n"
"v_add_u32 v1, v1, v5\n"
"v_lshrrev_b32 v5, 2, v0\n"
"v_add_u32 v5, s48, v5\n"
"v_and_b32 v5, 3, v5\n"
"v_lshl_add_u32 v4, v5, 4, v4\n"
"v_and_b32 v5, 2, v0\n"
"v_lshl_add_u32 v4, v5, 2, v4\n"
"v_lshrrev_b32 v5, 4, v0\n"
"v_lshl_add_u32 v4, v5, 1, v4\n"
"v_lshlrev_b32 v4, 4, v4\n"
"v_add_u32 v4, 5120, v4\n"
"v_lshlrev_b32 v7, 4, v0\n"
"s_mul_i32 s70, s48, 208\n"
"v_lshl_add_u32 v5, v0, 2, s70\n"
"v_mov_b64 v[36:37], 0\n"
"s_sleep 18\n"
"buffer_load_dwordx3 v[24:26], v4, s[40:43], s56 offen offset:0 sc1\n"
"s_sleep 10\n"
"buffer_load_dwordx4 v[8:11], v7, s[40:43], s62 offen offset:0 sc1\n"
"buffer_load_dwordx4 v[12:15], v7, s[40:43], s62 offen offset:1024 sc1\n"
"buffer_load_dwordx4 v[16:19], v7, s[40:43], s62 offen offset:2048 sc1\n"
"buffer_load_dwordx4 v[20:23], v7, s[40:43], s62 offen offset:3072 sc1\n"
".Lgb_194_%=:\n"
"s_waitcnt vmcnt(4)\n"
"v_max_f32_dpp v27, v24, v24 row_ror:1 row_mask:0xf bank_mask:0xf\n"
"s_nop 1\n"
"v_max_f32_dpp v27, v27, v27 row_ror:2 row_mask:0xf bank_mask:0xf\n"
"s_nop 1\n"
"v_max_f32_dpp v27, v27, v27 row_ror:4 row_mask:0xf bank_mask:0xf\n"
"s_nop 1\n"
"v_max_f32_dpp v27, v27, v27 row_ror:8 row_mask:0xf bank_mask:0xf\n"
"s_nop 1\n"
"v_max_f32_dpp v27, v27, v27 row_bcast:15 row_mask:0xa bank_mask:0xf\n"
"s_nop 1\n"
"v_max_f32_dpp v27, v27, v27 row_bcast:31 row_mask:0xc bank_mask:0xf\n"
"s_nop 0\n"
"v_readlane_b32 s70, v27, 63\n"
"s_nop 1\n"
"v_subrev_f32 v24, s70, v24\n"
"v_exp_f32 v24, v24\n"
"s_nop 0\n"
"v_cvt_pk_bf16_f32 v24, v24, 0\n"
"v_lshlrev_b32 v27, 16, v24\n"
"v_mul_f32 v27, v27, v25\n"
"v_and_b32_dpp v28, v24, v2 row_newbcast:0 row_mask:0xf bank_mask:0xf\n"
"v_and_b32_dpp v29, v24, v3 row_newbcast:0 row_mask:0xf bank_mask:0xf\n"
"v_add_f32_dpp v27, v27, v27 row_ror:1 row_mask:0xf bank_mask:0xf\n"
"v_and_b32_dpp v30, v24, v2 row_newbcast:1 row_mask:0xf bank_mask:0xf\n"
"v_and_b32_dpp v31, v24, v3 row_newbcast:1 row_mask:0xf bank_mask:0xf\n"
"v_add_f32_dpp v27, v27, v27 row_ror:2 row_mask:0xf bank_mask:0xf\n"
"v_and_b32_dpp v32, v24, v2 row_newbcast:2 row_mask:0xf bank_mask:0xf\n"
"v_and_b32_dpp v33, v24, v3 row_newbcast:2 row_mask:0xf bank_mask:0xf\n"
"v_add_f32_dpp v27, v27, v27 row_ror:4 row_mask:0xf bank_mask:0xf\n"
"v_and_b32_dpp v34, v24, v2 row_newbcast:3 row_mask:0xf bank_mask:0xf\n"
"v_and_b32_dpp v35, v24, v3 row_newbcast:3 row_mask:0xf bank_mask:0xf\n"
"v_add_f32_dpp v27, v27, v27 row_ror:8 row_mask:0xf bank_mask:0xf\n"
"s_nop 1\n"
"v_add_f32_dpp v27, v27, v27 row_bcast:15 row_mask:0xa bank_mask:0xf\n"
"s_nop 1\n"
"v_add_f32_dpp v27, v27, v27 row_bcast:31 row_mask:0xc bank_mask:0xf\n"
"v_rcp_f32 v27, v27\n"
"s_nop 0\n"
"v_readlane_b32 s70, v27, 63\n"
"s_waitcnt vmcnt(2)\n"
"v_smfmac_f32_16x16x64_bf16 v[36:39], v[28:31], v[8:15], v1\n"
"s_waitcnt vmcnt(0)\n"
"v_smfmac_f32_16x16x64_bf16 v[36:39], v[32:35], v[16:23], v1\n"
"v_bitop3_b32 v40, v11, v15, s66 bitop3:0x7e\n"
"v_bitop3_b32 v40, v19, v40, s66 bitop3:0xde\n"
"v_bitop3_b32 v40, v23, v40, s66 bitop3:0xde\n"
"v_bitop3_b32 v40, v26, v40, s66 bitop3:0xde\n"
"v_cmp_ne_u32 vcc, 0, v40\n"
"s_mov_b32 exec_hi, 0xffff\n"
"s_nop 1\n"
"v_mul_f32 v36, s70, v36\n"
"v_mul_f32 v37, s70, v37\n"
"v_cvt_pk_bf16_f32 v36, v36, v37\n"
"ds_write_b32 v5, v36 offset:3072\n"
"s_mov_b32 exec_hi, -1\n"
"s_cbranch_vccnz .Lgr_195_%=\n"
".Lgo_196_%=:\n"
"v_and_b32 v7, 3, v0\n"
"v_mul_u32_u24 v7, 208, v7\n"
"v_and_b32 v1, 48, v0\n"
"v_add_u32 v7, v7, v1\n"
"s_waitcnt lgkmcnt(0)\n"
"s_barrier\n"
"ds_read_b128 v[8:11], v7 offset:3072\n"
"ds_read_b128 v[12:15], v7 offset:3136\n"
"ds_read_b128 v[16:19], v7 offset:3200\n"
"s_add_u32 s66, s54, 10\n"
"s_lshl2_add_u32 s70, s50, s48\n"
"s_lshl_b32 s70, s70, 7\n"
"s_add_u32 s70, s70, s57\n"
"v_lshlrev_b32 v1, 3, v0\n"
"s_waitcnt lgkmcnt(2)\n"
"v_mfma_f32_16x16x32_bf16 v[20:23], v[8:11], a[144:147], 0\n"
"v_mfma_f32_16x16x32_bf16 v[24:27], v[8:11], a[156:159], 0\n"
"v_mfma_f32_16x16x32_bf16 v[28:31], v[8:11], a[168:171], 0\n"
"s_waitcnt lgkmcnt(1)\n"
"v_mfma_f32_16x16x32_bf16 v[20:23], v[12:15], a[148:151], v[20:23]\n"
"v_mfma_f32_16x16x32_bf16 v[24:27], v[12:15], a[160:163], v[24:27]\n"
"v_mfma_f32_16x16x32_bf16 v[28:31], v[12:15], a[172:175], v[28:31]\n"
"s_waitcnt lgkmcnt(0)\n"
"v_mfma_f32_16x16x32_bf16 v[20:23], v[16:19], a[152:155], v[20:23]\n"
"v_mfma_f32_16x16x32_bf16 v[24:27], v[16:19], a[164:167], v[24:27]\n"
"v_mfma_f32_16x16x32_bf16 v[28:31], v[16:19], a[176:179], v[28:31]\n"
"v_mov_b32 v35, s66\n"
"v_pk_add_f32 v[20:21], v[20:21], v[22:23]\n"
"v_add_f32 v20, v20, v21\n"
"s_nop 0\n"
"v_pk_add_f32 v[24:25], v[24:25], v[26:27]\n"
"v_add_f32 v24, v24, v25\n"
"s_nop 1\n"
"v_pk_add_f32 v[28:29], v[28:29], v[30:31]\n"
"v_add_f32 v28, v28, v29\n"
"v_mov_b32_dpp v21, v20 quad_perm:[1,1,3,3] row_mask:0xf bank_mask:0xf\n"
"v_mov_b32_dpp v25, v24 quad_perm:[1,1,3,3] row_mask:0xf bank_mask:0xf\n"
"v_mov_b32_dpp v29, v28 quad_perm:[1,1,3,3] row_mask:0xf bank_mask:0xf\n"
"s_mov_b64 exec, 0x5555\n"
"v_cvt_pk_bf16_f32 v32, v20, v21\n"
"v_cvt_pk_bf16_f32 v33, v24, v25\n"
"v_cvt_pk_bf16_f32 v34, v28, v29\n"
"buffer_store_dwordx4 v[32:35], v1, s[40:43], s70 offen offset:0 sc1\n"
"s_mov_b64 exec, -1\n"
"s_mul_i32 s75, s49, 160\n"
"s_add_u32 s75, s75, s58\n"
"s_lshl_b32 s78, s48, 5\n"
"s_add_u32 s75, s75, s78\n"
"s_add_u32 s75, s75, 393216\n"
"s_mov_b32 s78, 0x55555555\n"
"s_cmp_eq_u32 s48, 3\n"
"s_cselect_b32 s79, s78, 0\n"
"s_cmp_eq_u32 s48, 0\n"
"s_cbranch_scc0 .Lr_197_%=\n"
"v_and_b32 v38, 15, v0\n"
"v_lshlrev_b32 v38, 1, v38\n"
"s_mov_b32 s70, 0x44ee44\n"
"v_bfe_u32 v1, s70, v38, 2\n"
"s_mov_b32 s70, 0xf5a0a0\n"
"v_bfe_u32 v38, s70, v38, 2\n"
"v_mul_u32_u24 v1, 5, v1\n"
"v_lshlrev_b32 v2, 2, v38\n"
"v_lshlrev_b32 v1, v2, v1\n"
"v_lshlrev_b32 v38, v38, 1\n"
"v_bfe_i32 v2, v38, 0, 1\n"
"v_and_b32 v2, 0x3f80, v2\n"
"v_bfe_i32 v3, v38, 1, 1\n"
"v_and_b32 v3, 0x3f80, v3\n"
"v_bfe_i32 v4, v38, 2, 1\n"
"v_and_b32 v4, 0x3f80, v4\n"
"v_bfe_i32 v5, v38, 3, 1\n"
"v_and_b32 v5, 0x3f80, v5\n"
"v_and_b32 v7, 15, v0\n"
"v_bfe_u32 v38, v0, 4, 1\n"
"v_lshl_add_u32 v7, v7, 1, v38\n"
"v_and_b32 v38, 32, v0\n"
"v_add_lshl_u32 v7, v7, v38, 4\n"
"v_mov_b64 v[28:29], 0\n"
"v_mov_b64 v[30:31], 0\n"
"v_bfe_u32 v38, v0, 2, 2\n"
"v_lshrrev_b32 v36, 4, v0\n"
"v_mad_u32_u24 v38, v38, 3, v36\n"
"v_and_b32 v36, 3, v0\n"
"v_lshl_add_u32 v36, v38, 3, v36\n"
"v_lshlrev_b32 v36, 2, v36\n"
"v_lshrrev_b32 v42, 4, v0\n"
"v_and_b32 v38, 1, v0\n"
"v_lshl_add_u32 v42, v42, 1, v38\n"
"v_lshlrev_b32 v42, 4, v42\n"
".Lr_197_%=:\n"
"s_cmp_eq_u32 s48, 0\n"
"s_cbranch_scc0 .Lr_198_%=\n"
"s_sleep 7\n"
"buffer_load_dwordx4 v[8:11], v7, s[40:43], s57 offen offset:0 sc1\n"
"buffer_load_dwordx4 v[12:15], v7, s[40:43], s57 offen offset:1024 sc1\n"
"buffer_load_dwordx4 v[16:19], v7, s[40:43], s57 offen offset:2048 sc1\n"
"buffer_load_dwordx4 v[20:23], v7, s[40:43], s57 offen offset:3072 sc1\n"
".Lr_198_%=:\n"
"s_cmp_lg_u32 s48, 0\n"
"s_cbranch_scc0 .Lr_199_%=\n"
"v_lshrrev_b32 v42, 4, v0\n"
"v_and_b32 v38, 1, v0\n"
"v_lshl_add_u32 v42, v42, 1, v38\n"
"v_lshlrev_b32 v42, 4, v42\n"
".Lr_199_%=:\n"
"s_cmp_eq_u32 s48, 0\n"
"s_cbranch_scc0 .Lr_200_%=\n"
".Lgb_201_%=:\n"
"s_waitcnt vmcnt(2)\n"
"v_smfmac_f32_16x16x64_bf16 v[28:31], v[2:5], v[8:15], v1\n"
"s_waitcnt vmcnt(0)\n"
"v_smfmac_f32_16x16x64_bf16 v[28:31], v[2:5], v[16:23], v1\n"
"v_bitop3_b32 v38, v11, v15, s66 bitop3:0x7e\n"
"v_bitop3_b32 v38, v19, v38, s66 bitop3:0xde\n"
"v_bitop3_b32 v38, v23, v38, s66 bitop3:0xde\n"
"v_cmp_ne_u32 vcc, 0, v38\n"
"s_nop 3\n"
"v_cvt_pk_bf16_f32 v40, v28, v29\n"
"v_cvt_pk_bf16_f32 v41, v30, v31\n"
"s_mov_b32 exec_hi, 0xffff\n"
"ds_write2_b32 v36, v40, v41 offset0:96 offset1:100\n"
"s_mov_b64 exec, -1\n"
"s_cbranch_vccnz .Lgr_202_%=\n"
".Lgo_203_%=:\n"
".Lr_200_%=:\n"
"s_waitcnt lgkmcnt(0)\n"
"s_barrier\n"
"ds_read_b128 v[32:35], v42 offset:384\n"
"ds_read_b128 v[36:39], v42 offset:512\n"
"ds_read_b128 v[40:43], v42 offset:640\n"
"v_mov_b64 v[8:9], 0\n"
"v_mov_b64 v[2:3], 0\n"
"v_and_b32 v1, 15, v0\n"
"v_and_b32 v4, 32, v0\n"
"v_lshl_add_u32 v1, v1, 1, v4\n"
"v_and_b32 v4, 16, v0\n"
"v_lshl_add_u32 v1, v4, 9, v1\n"
"s_waitcnt lgkmcnt(2)\n"
"v_smfmac_f32_16x16x64_bf16 v[2:5], v[32:35], a[180:187], v6\n"
"v_smfmac_f32_16x16x64_bf16 v[8:11], v[32:35], a[204:211], v6\n"
"s_waitcnt lgkmcnt(1)\n"
"v_smfmac_f32_16x16x64_bf16 v[2:5], v[36:39], a[188:195], v6\n"
"v_smfmac_f32_16x16x64_bf16 v[8:11], v[36:39], a[212:219], v6\n"
"s_waitcnt lgkmcnt(0)\n"
"v_smfmac_f32_16x16x64_bf16 v[2:5], v[40:43], a[196:203], v6\n"
"v_smfmac_f32_16x16x64_bf16 v[8:11], v[40:43], a[220:227], v6\n"
"s_nop 3\n"
"v_add_f32 v12, v2, v3\n"
"s_nop 2\n"
"v_add_f32_dpp v12, v8, v9 quad_perm:[0,1,2,3] row_mask:0xc bank_mask:0xf\n"
"s_nop 1\n"
"v_mov_b32_dpp v2, v12 quad_perm:[1,1,3,3] row_mask:0xf bank_mask:0xf\n"
"s_mov_b64 exec, s[78:79]\n"
"v_cvt_pk_bf16_f32 v2, v12, v2\n"
"buffer_store_dword v2, v1, s[40:43], s75 offen offset:0 sc1\n"
"s_mov_b64 exec, -1\n"
"s_or_b32 s70, s49, s48\n"
"s_cbranch_scc1 .Le_204_%=\n"
"v_and_b32 v8, 15, v0\n"
"v_lshrrev_b32 v9, 4, v0\n"
"v_lshl_add_u32 v8, v8, 1, v9\n"
"v_lshlrev_b32 v8, 3, v8\n"
"v_lshlrev_b32 v8, 1, v8\n"
"ds_read_b128 v[40:43], v8 offset:2560\n"
"v_and_b32 v1, 3, v0\n"
"v_mul_u32_u24 v1, 0x5555, v1\n"
"v_bfe_i32 v3, v0, 2, 1\n"
"v_and_b32 v3, 0x3f80, v3\n"
"v_xor_b32 v2, 0x3f80, v3\n"
"v_mov_b64 v[4:5], v[2:3]\n"
"v_and_b32 v7, 15, v0\n"
"v_min_u32 v7, 9, v7\n"
"v_mul_u32_u24 v9, 160, v9\n"
"v_lshl_add_u32 v7, v7, 4, v9\n"
"s_add_u32 s73, s58, s59\n"
"s_add_u32 s73, s73, 393216\n"
"s_add_u32 s74, s73, 2560\n"
"s_mov_b32 s44, s68\n"
"s_mov_b32 s45, s69\n"
"s_mov_b32 s46, 1280\n"
"s_mov_b32 s47, 0x20000\n"
"s_mul_i32 s70, s50, 160\n"
"s_sleep 13\n"
"buffer_load_dwordx4 v[8:11], v7, s[40:43], s73 offen offset:0 sc1\n"
"buffer_load_dwordx4 v[12:15], v7, s[40:43], s73 offen offset:640 sc1\n"
"buffer_load_dwordx4 v[16:19], v7, s[40:43], s73 offen offset:1280 sc1\n"
"buffer_load_dwordx4 v[20:23], v7, s[40:43], s73 offen offset:1920 sc1\n"
"buffer_load_dwordx4 v[24:27], v7, s[40:43], s74 offen offset:0 sc1\n"
"buffer_load_dwordx4 v[28:31], v7, s[40:43], s74 offen offset:640 sc1\n"
"buffer_load_dwordx4 v[32:35], v7, s[40:43], s74 offen offset:1280 sc1\n"
"buffer_load_dwordx4 v[36:39], v7, s[40:43], s74 offen offset:1920 sc1\n"
".Lgb_205_%=:\n"
"s_waitcnt lgkmcnt(0)\n"
"v_add_f32 v40, s55, v40\n"
"v_add_f32 v41, s55, v41\n"
"v_add_f32 v42, s55, v42\n"
"v_add_f32 v43, s55, v43\n"
"s_waitcnt vmcnt(6)\n"
"s_nop 0\n"
"v_smfmac_f32_16x16x64_bf16 v[40:43], v[2:5], v[8:15], v1\n"
"v_max_u32 v6, v8, v9\n"
"v_max3_u32 v6, v6, v10, v11\n"
"v_max3_u32 v6, v6, v12, v13\n"
"v_max3_u32 v6, v6, v14, v15\n"
"s_waitcnt vmcnt(4)\n"
"v_smfmac_f32_16x16x64_bf16 v[40:43], v[2:5], v[16:23], v1\n"
"v_max3_u32 v6, v6, v16, v17\n"
"v_max3_u32 v6, v6, v18, v19\n"
"v_max3_u32 v6, v6, v20, v21\n"
"v_max3_u32 v6, v6, v22, v23\n"
"v_and_b32 v8, 15, v0\n"
"v_lshrrev_b32 v9, 4, v0\n"
"v_lshl_add_u32 v8, v8, 1, v9\n"
"v_lshlrev_b32 v8, 3, v8\n"
"s_waitcnt vmcnt(2)\n"
"v_smfmac_f32_16x16x64_bf16 v[40:43], v[2:5], v[24:31], v1\n"
"v_max3_u32 v6, v6, v24, v25\n"
"v_max3_u32 v6, v6, v26, v27\n"
"v_max3_u32 v6, v6, v28, v29\n"
"v_max3_u32 v6, v6, v30, v31\n"
"s_waitcnt vmcnt(0)\n"
"v_smfmac_f32_16x16x64_bf16 v[40:43], v[2:5], v[32:39], v1\n"
"v_max3_u32 v6, v6, v32, v33\n"
"v_max3_u32 v6, v6, v34, v35\n"
"v_max3_u32 v6, v6, v36, v37\n"
"v_max3_u32 v6, v6, v38, v39\n"
"v_cmp_eq_u32 vcc, -1, v6\n"
"s_nop 2\n"
"v_cvt_pk_bf16_f32 v10, v40, v41\n"
"v_cvt_pk_bf16_f32 v11, v42, v43\n"
"s_cbranch_vccnz .Lgr_206_%=\n"
".Lgo_207_%=:\n"
"s_mov_b64 exec, 0x03ff03ff\n"
"buffer_store_dwordx2 v[10:11], v8, s[44:47], s70 offen\n"
"s_mov_b64 exec, -1\n"
".Le_204_%=:\n"
"s_or_b32 s70, s2, s48\n"
"s_cbranch_scc1 .Le_208_%=\n"
"s_add_u32 s66, s54, 16\n"
"v_mov_b32 v1, s66\n"
"s_mov_b32 s70, 5390336\n"
"s_mov_b64 exec, 1\n"
"buffer_store_dword v1, off, s[40:43], s70 sc1\n"
"s_mov_b64 exec, -1\n"
".Le_208_%=:\n"
"s_endpgm\n"
".Lgr_21_%=:\n"
"s_nop 7\n"
"s_sub_u32 s65, s65, 1\n"
"s_cmp_lt_i32 s65, 0\n"
"s_cbranch_scc1 .Lgo_22_%=\n"
"buffer_load_dwordx4 v[24:27], v7, s[40:43], s73 offen offset:0 sc1\n"
"buffer_load_dwordx4 v[28:31], v7, s[40:43], s73 offen offset:1024 sc1\n"
"s_waitcnt vmcnt(0) lgkmcnt(0)\n"
"s_nop 7\n"
"s_branch .Lgb_20_%=\n"
".Lgr_39_%=:\n"
"s_nop 7\n"
"s_sub_u32 s65, s65, 1\n"
"s_cmp_lt_i32 s65, 0\n"
"s_cbranch_scc1 .Lgo_40_%=\n"
"v_mov_b64 v[28:29], 0\n"
"v_mov_b64 v[30:31], 0\n"
"buffer_load_dwordx4 v[8:11], v7, s[40:43], s57 offen offset:0 sc1\n"
"buffer_load_dwordx4 v[12:15], v7, s[40:43], s57 offen offset:1024 sc1\n"
"buffer_load_dwordx4 v[16:19], v7, s[40:43], s57 offen offset:2048 sc1\n"
"buffer_load_dwordx4 v[20:23], v7, s[40:43], s57 offen offset:3072 sc1\n"
"s_waitcnt vmcnt(0) lgkmcnt(0)\n"
"s_nop 7\n"
"s_branch .Lgb_38_%=\n"
".Lgr_44_%=:\n"
"s_nop 7\n"
"s_sub_u32 s65, s65, 1\n"
"s_cmp_lt_i32 s65, 0\n"
"s_cbranch_scc1 .Lgo_45_%=\n"
"v_mov_b64 v[28:29], 0\n"
"v_mov_b64 v[30:31], 0\n"
"buffer_load_dwordx4 v[8:11], v7, s[40:43], s56 offen offset:0 sc1\n"
"buffer_load_dwordx4 v[12:15], v7, s[40:43], s56 offen offset:1024 sc1\n"
"buffer_load_dwordx4 v[16:19], v7, s[40:43], s56 offen offset:2048 sc1\n"
"buffer_load_dwordx4 v[20:23], v7, s[40:43], s56 offen offset:3072 sc1\n"
"buffer_load_dwordx4 v[24:27], v37, s[40:43], s56 offen offset:0 sc1\n"
"s_waitcnt vmcnt(0) lgkmcnt(0)\n"
"s_nop 7\n"
"s_branch .Lgb_43_%=\n"
".Lgr_76_%=:\n"
"s_nop 7\n"
"s_sub_u32 s65, s65, 1\n"
"s_cmp_lt_i32 s65, 0\n"
"s_cbranch_scc1 .Lgo_77_%=\n"
"v_mov_b64 v[32:33], 0\n"
"v_mov_b64 v[34:35], 0\n"
"buffer_load_dwordx2 v[24:25], v6, s[40:43], s56 offen offset:0 sc1\n"
"buffer_load_dwordx2 v[28:29], v6, s[40:43], s56 offen offset:512 sc1\n"
"buffer_load_dwordx4 v[8:11], v7, s[40:43], s57 offen offset:0 sc1\n"
"buffer_load_dwordx4 v[12:15], v7, s[40:43], s57 offen offset:1024 sc1\n"
"buffer_load_dwordx4 v[16:19], v7, s[40:43], s57 offen offset:2048 sc1\n"
"buffer_load_dwordx4 v[20:23], v7, s[40:43], s57 offen offset:3072 sc1\n"
"buffer_load_dwordx2 v[26:27], v6, s[40:43], s56 offen offset:1024 sc1\n"
"buffer_load_dwordx2 v[30:31], v6, s[40:43], s56 offen offset:1536 sc1\n"
"s_waitcnt vmcnt(0) lgkmcnt(0)\n"
"s_nop 7\n"
"s_branch .Lgb_75_%=\n"
".Lgr_86_%=:\n"
"s_nop 7\n"
"s_sub_u32 s65, s65, 1\n"
"s_cmp_lt_i32 s65, 0\n"
"s_cbranch_scc1 .Lgo_87_%=\n"
"v_and_b32 v8, 15, v0\n"
"v_lshrrev_b32 v9, 4, v0\n"
"v_lshl_add_u32 v8, v8, 1, v9\n"
"v_lshlrev_b32 v8, 3, v8\n"
"v_lshlrev_b32 v8, 1, v8\n"
"ds_read_b128 v[40:43], v8 offset:2560\n"
"buffer_load_dwordx4 v[8:11], v7, s[40:43], s73 offen offset:0 sc1\n"
"buffer_load_dwordx4 v[12:15], v7, s[40:43], s73 offen offset:640 sc1\n"
"buffer_load_dwordx4 v[16:19], v7, s[40:43], s73 offen offset:1280 sc1\n"
"buffer_load_dwordx4 v[20:23], v7, s[40:43], s73 offen offset:1920 sc1\n"
"buffer_load_dwordx4 v[24:27], v7, s[40:43], s74 offen offset:0 sc1\n"
"buffer_load_dwordx4 v[28:31], v7, s[40:43], s74 offen offset:640 sc1\n"
"buffer_load_dwordx4 v[32:35], v7, s[40:43], s74 offen offset:1280 sc1\n"
"buffer_load_dwordx4 v[36:39], v7, s[40:43], s74 offen offset:1920 sc1\n"
"s_waitcnt vmcnt(0) lgkmcnt(0)\n"
"s_nop 7\n"
"s_branch .Lgb_85_%=\n"
".Lgr_100_%=:\n"
"s_nop 7\n"
"s_sub_u32 s65, s65, 1\n"
"s_cmp_lt_i32 s65, 0\n"
"s_cbranch_scc1 .Lgo_101_%=\n"
"v_mov_b64 v[28:29], 0\n"
"v_mov_b64 v[30:31], 0\n"
"buffer_load_dwordx4 v[8:11], v7, s[40:43], s57 offen offset:0 sc1\n"
"buffer_load_dwordx4 v[12:15], v7, s[40:43], s57 offen offset:1024 sc1\n"
"buffer_load_dwordx4 v[16:19], v7, s[40:43], s57 offen offset:2048 sc1\n"
"buffer_load_dwordx4 v[20:23], v7, s[40:43], s57 offen offset:3072 sc1\n"
"s_waitcnt vmcnt(0) lgkmcnt(0)\n"
"s_nop 7\n"
"s_branch .Lgb_99_%=\n"
".Lgr_104_%=:\n"
"s_nop 7\n"
"s_sub_u32 s65, s65, 1\n"
"s_cmp_lt_i32 s65, 0\n"
"s_cbranch_scc1 .Lgo_105_%=\n"
"v_mov_b64 v[28:29], 0\n"
"v_mov_b64 v[30:31], 0\n"
"buffer_load_dwordx4 v[8:11], v7, s[40:43], s56 offen offset:0 sc1\n"
"buffer_load_dwordx4 v[12:15], v7, s[40:43], s56 offen offset:1024 sc1\n"
"buffer_load_dwordx4 v[16:19], v7, s[40:43], s56 offen offset:2048 sc1\n"
"buffer_load_dwordx4 v[20:23], v7, s[40:43], s56 offen offset:3072 sc1\n"
"buffer_load_dwordx4 v[24:27], v37, s[40:43], s56 offen offset:0 sc1\n"
"s_waitcnt vmcnt(0) lgkmcnt(0)\n"
"s_nop 7\n"
"s_branch .Lgb_103_%=\n"
".Lgr_115_%=:\n"
"s_nop 7\n"
"s_sub_u32 s65, s65, 1\n"
"s_cmp_lt_i32 s65, 0\n"
"s_cbranch_scc1 .Lgo_116_%=\n"
"buffer_load_dwordx4 v[8:11], v7, s[40:43], s60 offen offset:0 sc1\n"
"buffer_load_dwordx4 v[12:15], v7, s[40:43], s60 offen offset:128 sc1\n"
"buffer_load_dwordx4 v[16:19], v7, s[40:43], s60 offen offset:256 sc1\n"
"s_waitcnt vmcnt(0) lgkmcnt(0)\n"
"s_nop 7\n"
"s_branch .Lgb_114_%=\n"
".Lgr_123_%=:\n"
"s_nop 7\n"
"s_sub_u32 s65, s65, 1\n"
"s_cmp_lt_i32 s65, 0\n"
"s_cbranch_scc1 .Lgo_124_%=\n"
"v_mov_b64 v[36:37], 0\n"
"buffer_load_dwordx3 v[24:26], v4, s[40:43], s56 offen offset:0 sc1\n"
"buffer_load_dwordx4 v[8:11], v7, s[40:43], s62 offen offset:0 sc1\n"
"buffer_load_dwordx4 v[12:15], v7, s[40:43], s62 offen offset:1024 sc1\n"
"buffer_load_dwordx4 v[16:19], v7, s[40:43], s62 offen offset:2048 sc1\n"
"buffer_load_dwordx4 v[20:23], v7, s[40:43], s62 offen offset:3072 sc1\n"
"s_waitcnt vmcnt(0) lgkmcnt(0)\n"
"s_nop 7\n"
"s_branch .Lgb_122_%=\n"
".Lgr_138_%=:\n"
"s_nop 7\n"
"s_sub_u32 s65, s65, 1\n"
"s_cmp_lt_i32 s65, 0\n"
"s_cbranch_scc1 .Lgo_139_%=\n"
"v_mov_b64 v[28:29], 0\n"
"v_mov_b64 v[30:31], 0\n"
"buffer_load_dwordx4 v[8:11], v7, s[40:43], s57 offen offset:0 sc1\n"
"buffer_load_dwordx4 v[12:15], v7, s[40:43], s57 offen offset:1024 sc1\n"
"buffer_load_dwordx4 v[16:19], v7, s[40:43], s57 offen offset:2048 sc1\n"
"buffer_load_dwordx4 v[20:23], v7, s[40:43], s57 offen offset:3072 sc1\n"
"s_waitcnt vmcnt(0) lgkmcnt(0)\n"
"s_nop 7\n"
"s_branch .Lgb_137_%=\n"
".Lgr_147_%=:\n"
"s_nop 7\n"
"s_sub_u32 s65, s65, 1\n"
"s_cmp_lt_i32 s65, 0\n"
"s_cbranch_scc1 .Lgo_148_%=\n"
"v_and_b32 v8, 15, v0\n"
"v_lshrrev_b32 v9, 4, v0\n"
"v_lshl_add_u32 v8, v8, 1, v9\n"
"v_lshlrev_b32 v8, 3, v8\n"
"v_lshlrev_b32 v8, 1, v8\n"
"ds_read_b128 v[40:43], v8 offset:2560\n"
"buffer_load_dwordx4 v[8:11], v7, s[40:43], s73 offen offset:0 sc1\n"
"buffer_load_dwordx4 v[12:15], v7, s[40:43], s73 offen offset:640 sc1\n"
"buffer_load_dwordx4 v[16:19], v7, s[40:43], s73 offen offset:1280 sc1\n"
"buffer_load_dwordx4 v[20:23], v7, s[40:43], s73 offen offset:1920 sc1\n"
"buffer_load_dwordx4 v[24:27], v7, s[40:43], s74 offen offset:0 sc1\n"
"buffer_load_dwordx4 v[28:31], v7, s[40:43], s74 offen offset:640 sc1\n"
"buffer_load_dwordx4 v[32:35], v7, s[40:43], s74 offen offset:1280 sc1\n"
"buffer_load_dwordx4 v[36:39], v7, s[40:43], s74 offen offset:1920 sc1\n"
"s_waitcnt vmcnt(0) lgkmcnt(0)\n"
"s_nop 7\n"
"s_branch .Lgb_146_%=\n"
".Lgr_156_%=:\n"
"s_nop 7\n"
"s_sub_u32 s65, s65, 1\n"
"s_cmp_lt_i32 s65, 0\n"
"s_cbranch_scc1 .Lgo_157_%=\n"
"v_mov_b64 v[28:29], 0\n"
"v_mov_b64 v[30:31], 0\n"
"buffer_load_dwordx4 v[8:11], v7, s[40:43], s56 offen offset:0 sc1\n"
"buffer_load_dwordx4 v[12:15], v7, s[40:43], s56 offen offset:1024 sc1\n"
"buffer_load_dwordx4 v[16:19], v7, s[40:43], s56 offen offset:2048 sc1\n"
"buffer_load_dwordx4 v[20:23], v7, s[40:43], s56 offen offset:3072 sc1\n"
"buffer_load_dwordx4 v[24:27], v37, s[40:43], s56 offen offset:0 sc1\n"
"s_waitcnt vmcnt(0) lgkmcnt(0)\n"
"s_nop 7\n"
"s_branch .Lgb_155_%=\n"
".Lgr_168_%=:\n"
"s_nop 7\n"
"s_sub_u32 s65, s65, 1\n"
"s_cmp_lt_i32 s65, 0\n"
"s_cbranch_scc1 .Lgo_169_%=\n"
"v_mov_b64 v[32:33], 0\n"
"v_mov_b64 v[34:35], 0\n"
"buffer_load_dwordx2 v[24:25], v6, s[40:43], s56 offen offset:0 sc1\n"
"buffer_load_dwordx2 v[28:29], v6, s[40:43], s56 offen offset:512 sc1\n"
"buffer_load_dwordx4 v[8:11], v7, s[40:43], s57 offen offset:0 sc1\n"
"buffer_load_dwordx4 v[12:15], v7, s[40:43], s57 offen offset:1024 sc1\n"
"buffer_load_dwordx4 v[16:19], v7, s[40:43], s57 offen offset:2048 sc1\n"
"buffer_load_dwordx4 v[20:23], v7, s[40:43], s57 offen offset:3072 sc1\n"
"buffer_load_dwordx2 v[26:27], v6, s[40:43], s56 offen offset:1024 sc1\n"
"buffer_load_dwordx2 v[30:31], v6, s[40:43], s56 offen offset:1536 sc1\n"
"s_waitcnt vmcnt(0) lgkmcnt(0)\n"
"s_nop 7\n"
"s_branch .Lgb_167_%=\n"
".Lgr_177_%=:\n"
"s_nop 7\n"
"s_sub_u32 s65, s65, 1\n"
"s_cmp_lt_i32 s65, 0\n"
"s_cbranch_scc1 .Lgo_178_%=\n"
"v_and_b32 v8, 15, v0\n"
"v_lshrrev_b32 v9, 4, v0\n"
"v_lshl_add_u32 v8, v8, 1, v9\n"
"v_lshlrev_b32 v8, 3, v8\n"
"v_lshlrev_b32 v8, 1, v8\n"
"ds_read_b128 v[40:43], v8 offset:2560\n"
"buffer_load_dwordx4 v[8:11], v7, s[40:43], s73 offen offset:0 sc1\n"
"buffer_load_dwordx4 v[12:15], v7, s[40:43], s73 offen offset:640 sc1\n"
"buffer_load_dwordx4 v[16:19], v7, s[40:43], s73 offen offset:1280 sc1\n"
"buffer_load_dwordx4 v[20:23], v7, s[40:43], s73 offen offset:1920 sc1\n"
"buffer_load_dwordx4 v[24:27], v7, s[40:43], s74 offen offset:0 sc1\n"
"buffer_load_dwordx4 v[28:31], v7, s[40:43], s74 offen offset:640 sc1\n"
"buffer_load_dwordx4 v[32:35], v7, s[40:43], s74 offen offset:1280 sc1\n"
"buffer_load_dwordx4 v[36:39], v7, s[40:43], s74 offen offset:1920 sc1\n"
"s_waitcnt vmcnt(0) lgkmcnt(0)\n"
"s_nop 7\n"
"s_branch .Lgb_176_%=\n"
".Lgr_186_%=:\n"
"s_nop 7\n"
"s_sub_u32 s65, s65, 1\n"
"s_cmp_lt_i32 s65, 0\n"
"s_cbranch_scc1 .Lgo_187_%=\n"
"v_mov_b64 v[28:29], 0\n"
"v_mov_b64 v[30:31], 0\n"
"buffer_load_dwordx4 v[8:11], v7, s[40:43], s56 offen offset:0 sc1\n"
"buffer_load_dwordx4 v[12:15], v7, s[40:43], s56 offen offset:1024 sc1\n"
"buffer_load_dwordx4 v[16:19], v7, s[40:43], s56 offen offset:2048 sc1\n"
"buffer_load_dwordx4 v[20:23], v7, s[40:43], s56 offen offset:3072 sc1\n"
"buffer_load_dwordx4 v[24:27], v37, s[40:43], s56 offen offset:0 sc1\n"
"s_waitcnt vmcnt(0) lgkmcnt(0)\n"
"s_nop 7\n"
"s_branch .Lgb_185_%=\n"
".Lgr_191_%=:\n"
"s_nop 7\n"
"s_sub_u32 s65, s65, 1\n"
"s_cmp_lt_i32 s65, 0\n"
"s_cbranch_scc1 .Lgo_192_%=\n"
"buffer_load_dwordx4 v[8:11], v7, s[40:43], s60 offen offset:0 sc1\n"
"buffer_load_dwordx4 v[12:15], v7, s[40:43], s60 offen offset:128 sc1\n"
"buffer_load_dwordx4 v[16:19], v7, s[40:43], s60 offen offset:256 sc1\n"
"s_waitcnt vmcnt(0) lgkmcnt(0)\n"
"s_nop 7\n"
"s_branch .Lgb_190_%=\n"
".Lgr_195_%=:\n"
"s_nop 7\n"
"s_sub_u32 s65, s65, 1\n"
"s_cmp_lt_i32 s65, 0\n"
"s_cbranch_scc1 .Lgo_196_%=\n"
"v_mov_b64 v[36:37], 0\n"
"buffer_load_dwordx3 v[24:26], v4, s[40:43], s56 offen offset:0 sc1\n"
"buffer_load_dwordx4 v[8:11], v7, s[40:43], s62 offen offset:0 sc1\n"
"buffer_load_dwordx4 v[12:15], v7, s[40:43], s62 offen offset:1024 sc1\n"
"buffer_load_dwordx4 v[16:19], v7, s[40:43], s62 offen offset:2048 sc1\n"
"buffer_load_dwordx4 v[20:23], v7, s[40:43], s62 offen offset:3072 sc1\n"
"s_waitcnt vmcnt(0) lgkmcnt(0)\n"
"s_nop 7\n"
"s_branch .Lgb_194_%=\n"
".Lgr_202_%=:\n"
"s_nop 7\n"
"s_sub_u32 s65, s65, 1\n"
"s_cmp_lt_i32 s65, 0\n"
"s_cbranch_scc1 .Lgo_203_%=\n"
"v_mov_b64 v[28:29], 0\n"
"v_mov_b64 v[30:31], 0\n"
"buffer_load_dwordx4 v[8:11], v7, s[40:43], s57 offen offset:0 sc1\n"
"buffer_load_dwordx4 v[12:15], v7, s[40:43], s57 offen offset:1024 sc1\n"
"buffer_load_dwordx4 v[16:19], v7, s[40:43], s57 offen offset:2048 sc1\n"
"buffer_load_dwordx4 v[20:23], v7, s[40:43], s57 offen offset:3072 sc1\n"
"s_waitcnt vmcnt(0) lgkmcnt(0)\n"
"s_nop 7\n"
"s_branch .Lgb_201_%=\n"
".Lgr_206_%=:\n"
"s_nop 7\n"
"s_sub_u32 s65, s65, 1\n"
"s_cmp_lt_i32 s65, 0\n"
"s_cbranch_scc1 .Lgo_207_%=\n"
"v_and_b32 v8, 15, v0\n"
"v_lshrrev_b32 v9, 4, v0\n"
"v_lshl_add_u32 v8, v8, 1, v9\n"
"v_lshlrev_b32 v8, 3, v8\n"
"v_lshlrev_b32 v8, 1, v8\n"
"ds_read_b128 v[40:43], v8 offset:2560\n"
"buffer_load_dwordx4 v[8:11], v7, s[40:43], s73 offen offset:0 sc1\n"
"buffer_load_dwordx4 v[12:15], v7, s[40:43], s73 offen offset:640 sc1\n"
"buffer_load_dwordx4 v[16:19], v7, s[40:43], s73 offen offset:1280 sc1\n"
"buffer_load_dwordx4 v[20:23], v7, s[40:43], s73 offen offset:1920 sc1\n"
"buffer_load_dwordx4 v[24:27], v7, s[40:43], s74 offen offset:0 sc1\n"
"buffer_load_dwordx4 v[28:31], v7, s[40:43], s74 offen offset:640 sc1\n"
"buffer_load_dwordx4 v[32:35], v7, s[40:43], s74 offen offset:1280 sc1\n"
"buffer_load_dwordx4 v[36:39], v7, s[40:43], s74 offen offset:1920 sc1\n"
"s_waitcnt vmcnt(0) lgkmcnt(0)\n"
"s_nop 7\n"
"s_branch .Lgb_205_%=\n"
".Lcode1_%=:\n"
  : "+{v0}"(tid), "+{s[0:1]}"(ka), "+{s2}"(bid) :
  : "memory", "vcc", "scc", "v1", "v2", "v3", "v4", "v5", "v6", "v7", "v8", "v9", "v10", "v11", "v12", "v13", "v14", "v15", "v16", "v17", "v18", "v19", "v20", "v21", "v22", "v23", "v24", "v25", "v26", "v27", "v28", "v29", "v30", "v31", "v32", "v33", "v34", "v35", "v36", "v37", "v38", "v39", "v40", "v41", "v42", "v43", "v44", "v45", "v46", "v47", "v48", "v49", "v50", "v51", "v52", "v53", "v54", "v55", "v56", "v57", "v58", "v59", "v60", "v61", "v62", "v63", "v64", "v65", "v66", "v67", "v68", "v69", "v70", "v71", "v72", "v73", "v74", "v75", "v76", "v77", "v78", "v79", "v80", "v81", "v82", "v83", "v84", "v85", "v86", "v87", "v88", "v89", "v90", "v91", "v92", "v93", "v94", "v95", "v96", "v97", "v98", "v99", "v100", "v101", "v102", "v103", "v104", "v105", "v106", "v107", "v108", "v109", "v110", "v111", "v112", "v113", "v114", "v115", "v116", "v117", "v118", "v119", "v120", "v121", "v122", "v123", "v124", "v125", "v126", "v127", "v128", "v129", "v130", "v131", "v132", "v133", "v134", "v135", "v136", "v137", "v138", "v139", "v140", "v141", "v142", "v143", "v144", "v145", "v146", "v147", "v148", "v149", "v150", "v151", "v152", "v153", "v154", "v155", "v156", "v157", "v158", "v159", "v160", "v161", "v162", "v163", "v164", "v165", "v166", "v167", "v168", "v169", "v170", "v171", "v172", "v173", "v174", "v175", "v176", "v177", "v178", "v179", "v180", "v181", "v182", "v183", "v184", "v185", "v186", "v187", "v188", "v189", "v190", "v191", "v192", "v193", "v194", "v195", "v196", "v197", "v198", "v199", "v200", "v201", "v202", "v203", "v204", "v205", "v206", "v207", "v208", "v209", "v210", "v211", "v212", "v213", "v214", "v215", "v216", "v217", "v218", "v219", "v220", "v221", "v222", "v223", "v224", "v225", "v226", "v227", "v228", "v229", "v230", "v231", "v232", "v233", "v234", "v235", "v236", "v237", "v238", "v239", "v240", "v241", "v242", "v243", "v244", "v245", "v246", "v247", "v248", "v249", "v250", "v251", "v252", "v253", "v254", "v255", "a0", "a1", "a2", "a3", "a4", "a5", "a6", "a7", "a8", "a9", "a10", "a11", "a12", "a13", "a14", "a15", "a16", "a17", "a18", "a19", "a20", "a21", "a22", "a23", "a24", "a25", "a26", "a27", "a28", "a29", "a30", "a31", "a32", "a33", "a34", "a35", "a36", "a37", "a38", "a39", "a40", "a41", "a42", "a43", "a44", "a45", "a46", "a47", "a48", "a49", "a50", "a51", "a52", "a53", "a54", "a55", "a56", "a57", "a58", "a59", "a60", "a61", "a62", "a63", "a64", "a65", "a66", "a67", "a68", "a69", "a70", "a71", "a72", "a73", "a74", "a75", "a76", "a77", "a78", "a79", "a80", "a81", "a82", "a83", "a84", "a85", "a86", "a87", "a88", "a89", "a90", "a91", "a92", "a93", "a94", "a95", "a96", "a97", "a98", "a99", "a100", "a101", "a102", "a103", "a104", "a105", "a106", "a107", "a108", "a109", "a110", "a111", "a112", "a113", "a114", "a115", "a116", "a117", "a118", "a119", "a120", "a121", "a122", "a123", "a124", "a125", "a126", "a127", "a128", "a129", "a130", "a131", "a132", "a133", "a134", "a135", "a136", "a137", "a138", "a139", "a140", "a141", "a142", "a143", "a144", "a145", "a146", "a147", "a148", "a149", "a150", "a151", "a152", "a153", "a154", "a155", "a156", "a157", "a158", "a159", "a160", "a161", "a162", "a163", "a164", "a165", "a166", "a167", "a168", "a169", "a170", "a171", "a172", "a173", "a174", "a175", "a176", "a177", "a178", "a179", "a180", "a181", "a182", "a183", "a184", "a185", "a186", "a187", "a188", "a189", "a190", "a191", "a192", "a193", "a194", "a195", "a196", "a197", "a198", "a199", "a200", "a201", "a202", "a203", "a204", "a205", "a206", "a207", "a208", "a209", "a210", "a211", "a212", "a213", "a214", "a215", "a216", "a217", "a218", "a219", "a220", "a221", "a222", "a223", "a224", "a225", "a226", "a227", "a228", "a229", "a230", "a231", "a232", "a233", "a234", "a235", "a236", "a237", "a238", "a239", "a240", "a241", "a242", "a243", "a244", "a245", "a246", "a247", "a248", "a249", "a250", "a251", "a252", "a253", "a254", "a255", "s3", "s4", "s5", "s6", "s7", "s8", "s9", "s10", "s11", "s12", "s13", "s14", "s15", "s16", "s17", "s18", "s19", "s20", "s21", "s22", "s23", "s24", "s25", "s26", "s27", "s28", "s29", "s30", "s31", "s32", "s33", "s34", "s35", "s36", "s37", "s38", "s39", "s40", "s41", "s42", "s43", "s44", "s45", "s46", "s47", "s48", "s49", "s50", "s51", "s52", "s53", "s54", "s55", "s56", "s57", "s58", "s59", "s60", "s61", "s62", "s63", "s64", "s65", "s66", "s67", "s68", "s69", "s70", "s71", "s72", "s73", "s74", "s75", "s76", "s77", "s78", "s79", "s80", "s81", "s82", "s83", "s84", "s85", "s86", "s87", "s88", "s89", "s90", "s91", "s92", "s93", "s94", "s95", "s96", "s97", "s98", "s99", "s100", "s101");
}
extern "C" void kernel_launch(void* const* d_in, const int* in_sizes, int n_in, void* d_out, int out_size,
                              void* d_ws, size_t ws_size, hipStream_t stream) {
  Args a;
  a.p[0] = d_in[0]; a.p[1] = d_in[1]; a.p[2] = d_in[2]; a.p[3] = d_in[3]; a.p[4] = d_in[4]; a.p[5] = d_in[6];
  a.p[6] = d_in[7]; a.p[7] = d_in[8]; a.p[8] = d_in[9]; a.p[9] = d_in[10]; a.p[10] = d_out;
  a.p[11] = d_ws;
  (void)hipFuncSetAttribute((const void*)decode_step, hipFuncAttributeMaxDynamicSharedMemorySize, 163840);
  hipLaunchKernelGGL(decode_step, dim3(256), dim3(256), 163840, stream, a);
}
```
